```python
import torch

def _softmax(x, axis=-1):
    return torch.softmax(x, dim=axis)
def _gelu(x):
    return torch.nn.functional.gelu(x, approximate="tanh")
def _silu(x):
    return torch.nn.functional.silu(x)
def _sigmoid(x):
    return torch.sigmoid(x)
def _layernorm(x, axis=-1, eps=1e-5):
    m = x.mean(dim=axis, keepdim=True)
    v = x.var(dim=axis, keepdim=True, unbiased=False)
    return (x - m) / torch.sqrt(v + eps)
def _scatter_add(tgt, idx, src, axis=0):
    return tgt.clone().index_add_(axis, idx, src)
def _segment_sum(src, seg, n):
    out = torch.zeros((n,) + src.shape[1:], dtype=src.dtype)
    return out.index_add_(0, seg, src)
def _pad(x, pw):
    flat = tuple(v for pair in reversed(pw) for v in pair)
    return torch.nn.functional.pad(x, flat)

kernel_name = 'sequential_preLN__ssm_conv_s4__glu_gated_bilinear__prompt'

def setup_inputs():

    _w = (lambda a: (lambda t: t.to(torch.bfloat16) if t.is_floating_point() else t)(torch.as_tensor(a)))
    def _A_setup():
        B, L, D, N = 1, 2048, 512, 128
        A_re = -(lambda _x: torch.exp(torch.as_tensor(_x)))((lambda *s: torch.randn(*s, dtype=torch.bfloat16) * 6.4)(D, N))
        A_im = torch.pi * (lambda *s: torch.randn(*s, dtype=torch.bfloat16) * 0.05)(D, N)
        B_re = (lambda *s: torch.randn(*s, dtype=torch.bfloat16) * 0.1)(D, N)
        B_im = (lambda *s: torch.randn(*s, dtype=torch.bfloat16) * 0.1)(D, N)
        C_re = (lambda *s: torch.randn(*s, dtype=torch.bfloat16) * 0.1)(D, N)
        C_im = (lambda *s: torch.randn(*s, dtype=torch.bfloat16) * 0.1)(D, N)
        log_dt = (lambda *s: torch.randn(*s, dtype=torch.bfloat16) * 0.05)(D) * 0.5 - 4.0
        Dskip = (lambda *s: torch.randn(*s, dtype=torch.bfloat16) * 0.05)(D)
        return {
            "u": _w((lambda *s: torch.randn(*s, dtype=torch.bfloat16) * 0.05)(B, L, D)),
            "A_re": _w(A_re),
            "A_im": _w(A_im),
            "B_re": _w(B_re),
            "B_im": _w(B_im),
            "C_re": _w(C_re),
            "C_im": _w(C_im),
            "log_dt": _w(log_dt),
            "Dskip": _w(Dskip),
        }
    _A_in = _A_setup()

    def _B_setup():
        B, S, D, F = 1, 2048, 512, 1792
        return {
            "x": _w((lambda *s: torch.randn(*s, dtype=torch.bfloat16) * 0.05)(B, S, D)),
            "w_gate": _w((lambda *s: torch.randn(*s, dtype=torch.bfloat16) * 0.4)(D, F) * (D ** -0.5)),
            "w_up": _w((lambda *s: torch.randn(*s, dtype=torch.bfloat16) * 0.4)(D, F) * (D ** -0.5)),
            "w_down": _w((lambda *s: torch.randn(*s, dtype=torch.bfloat16) * 0.4)(F, D) * (F ** -0.5)),
        }
    _B_in = _B_setup()

    return {
        "A_u": _A_in["u"],
        "A_A_re": _A_in["A_re"],
        "A_A_im": _A_in["A_im"],
        "A_B_re": _A_in["B_re"],
        "A_B_im": _A_in["B_im"],
        "A_C_re": _A_in["C_re"],
        "A_C_im": _A_in["C_im"],
        "A_log_dt": _A_in["log_dt"],
        "A_Dskip": _A_in["Dskip"],
        "B_x": _B_in["x"],
        "B_w_gate": _B_in["w_gate"],
        "B_w_up": _B_in["w_up"],
        "B_w_down": _B_in["w_down"],
    }

def reference(A_u, A_A_re, A_A_im, A_B_re, A_B_im, A_C_re, A_C_im, A_log_dt, A_Dskip, B_x, B_w_gate, B_w_up, B_w_down):
    def _A_ref(u, A_re, A_im, B_re, B_im, C_re, C_im, log_dt, Dskip):
        Bsz, L, D = u.shape
        dt = torch.exp(log_dt)[:, None]
        A = A_re + 1j * A_im
        Bc = B_re + 1j * B_im
        Cc = C_re + 1j * C_im
        logdA = dt * A
        dB = dt * Bc
        l = (lambda x, dt: x.to(dt))(torch.arange(L), A_re.dtype)
        P = torch.exp(logdA[:, :, None] * l[None, None, :])
        K = torch.einsum("dn,dnl,dn->dl", Cc, P, dB).real
        L2 = 2 * L
        Kp = _pad(K, ((0, 0), (0, L)))
        up = (lambda x, *d: x.permute(*d))(u, 0, 2, 1)
        up = _pad(up, ((0, 0), (0, 0), (0, L)))
        Kf = (lambda x, axes=(-2,-1): torch.fft.rfft2(x.float(), dim=axes))(Kp[:, None, :], axes=(-2, -1))
        Uf = (lambda x, axes=(-2,-1): torch.fft.rfft2(x.float(), dim=axes))(up[:, :, None, :], axes=(-2, -1))
        Yf = Uf * Kf[None, :, :, :]
        y = (lambda x, s, axes=(-2,-1): torch.fft.irfft2(x, s=s, dim=axes).to(torch.bfloat16))(Yf, (1, L2), axes=(-2, -1))[:, :, 0, :L]
        y = (lambda x, *d: x.permute(*d))(y, 0, 2, 1) + u * Dskip[None, None, :]
        return y

    def _B_ref(x, w_gate, w_up, w_down):
        g = torch.einsum("bsd,df->bsf", x, w_gate)
        u = torch.einsum("bsd,df->bsf", x, w_up)
        h = g * u
        return torch.einsum("bsf,fd->bsd", h, w_down)

    x = A_u
    h = x + _A_ref(_layernorm(x, axis=-1), A_A_re, A_A_im, A_B_re, A_B_im, A_C_re, A_C_im, A_log_dt, A_Dskip)
    h2 = h + _B_ref(_layernorm(h, axis=-1), B_w_gate, B_w_up, B_w_down)
    return h2
```

```cpp
#include <hip/hip_runtime.h>
#include <hip/hip_bf16.h>
#include <stdint.h>
typedef unsigned short u16;
static constexpr int L=2048, D=512, NS=128, F=1792;
__device__ __forceinline__ float bf2f(u16 v){ return __uint_as_float(((unsigned)v)<<16); }
__device__ __forceinline__ u16 f2bf(float f){ unsigned u=__float_as_uint(f); u += 0x7fffu + ((u>>16)&1u); return (u16)(u>>16); }
__device__ __forceinline__ float rbf(float f){ return bf2f(f2bf(f)); }

__global__ void ln_kernel(const u16* x, u16* y){
  int row=blockIdx.x, tid=threadIdx.x;
  float v[8]; float s=0;
  for(int i=0;i<8;i++){ v[i]=bf2f(x[row*D+tid*8+i]); s+=v[i]; }
  for(int o=32;o>0;o>>=1) s+=__shfl_xor(s,o);
  float m=s/D; float q=0;
  for(int i=0;i<8;i++){ float d=v[i]-m; q+=d*d; }
  for(int o=32;o>0;o>>=1) q+=__shfl_xor(q,o);
  float mb=rbf(m), vb=rbf(q/D); float sd=rbf(sqrtf(rbf(vb+1e-5f)));
  for(int i=0;i<8;i++) y[row*D+tid*8+i]=f2bf(rbf(v[i]-mb)/sd);
}
__global__ void kgen_kernel(const u16* Are,const u16* Aim,const u16* Bre,const u16* Bim,const u16* Cre,const u16* Cim,const u16* ldt,float* K){
  int d=blockIdx.x;
  __shared__ float ar[NS],ai[NS],wr[NS],wi[NS];
  float dt=rbf(expf(bf2f(ldt[d])));
  for(int n=threadIdx.x;n<NS;n+=blockDim.x){
    ar[n]=dt*bf2f(Are[d*NS+n]); ai[n]=dt*bf2f(Aim[d*NS+n]);
    float br=dt*bf2f(Bre[d*NS+n]), bi=dt*bf2f(Bim[d*NS+n]);
    float cr=bf2f(Cre[d*NS+n]), ci=bf2f(Cim[d*NS+n]);
    wr[n]=cr*br-ci*bi; wi[n]=cr*bi+ci*br;
  }
  __syncthreads();
  for(int l=threadIdx.x;l<L;l+=blockDim.x){
    float lf=rbf((float)l); float s=0;
    for(int n=0;n<NS;n++){ float e=expf(ar[n]*lf); float sn,cs; sincosf(ai[n]*lf,&sn,&cs); s+=e*(wr[n]*cs-wi[n]*sn); }
    K[d*L+l]=s;
  }
}
__global__ void conv_kernel(const u16* x,const u16* u,const float* K,const u16* Dsk,u16* h){
  int d=blockIdx.x;
  __shared__ float ks[L], us[L];
  for(int i=threadIdx.x;i<L;i+=blockDim.x){ ks[i]=K[d*L+i]; us[i]=bf2f(u[i*D+d]); }
  __syncthreads();
  float dk=bf2f(Dsk[d]);
  for(int t=threadIdx.x;t<L;t+=blockDim.x){
    float s=0; for(int j=0;j<=t;j++) s+=ks[j]*us[t-j];
    float y=rbf(rbf(s)+rbf(us[t]*dk));
    h[t*D+d]=f2bf(bf2f(x[t*D+d])+y);
  }
}
__global__ void gemm1_kernel(const u16* a,const u16* wg,const u16* wu,u16* H){
  int t=blockIdx.x; __shared__ float as[D];
  for(int i=threadIdx.x;i<D;i+=blockDim.x) as[i]=bf2f(a[t*D+i]);
  __syncthreads();
  for(int f=threadIdx.x;f<F;f+=blockDim.x){
    float g=0,u=0; for(int k=0;k<D;k++){ g+=as[k]*bf2f(wg[k*F+f]); u+=as[k]*bf2f(wu[k*F+f]); }
    H[t*F+f]=f2bf(rbf(g)*rbf(u));
  }
}
__global__ void gemm2_kernel(const u16* H,const u16* wd,const u16* h,u16* out){
  int t=blockIdx.x; __shared__ float as[F];
  for(int i=threadIdx.x;i<F;i+=blockDim.x) as[i]=bf2f(H[t*F+i]);
  __syncthreads();
  for(int c=threadIdx.x;c<D;c+=blockDim.x){
    float g=0; for(int k=0;k<F;k++) g+=as[k]*bf2f(wd[k*D+c]);
    out[t*D+c]=f2bf(bf2f(h[t*D+c])+rbf(g));
  }
}
extern "C" void kernel_launch(void* const* d_in, const int* in_sizes, int n_in, void* d_out, int out_size, void* d_ws, size_t ws_size, hipStream_t stream){
  const u16* x=(const u16*)d_in[0];
  char* ws=(char*)d_ws;
  u16* u=(u16*)ws; u16* h=(u16*)(ws+(4<<20)); u16* l2=(u16*)(ws+(8<<20)); float* K=(float*)(ws+(12<<20)); u16* H=(u16*)(ws+(20<<20));
  ln_kernel<<<L,64,0,stream>>>(x,u);
  kgen_kernel<<<D,256,0,stream>>>((const u16*)d_in[1],(const u16*)d_in[2],(const u16*)d_in[3],(const u16*)d_in[4],(const u16*)d_in[5],(const u16*)d_in[6],(const u16*)d_in[7],K);
  conv_kernel<<<D,256,0,stream>>>(x,u,K,(const u16*)d_in[8],h);
  ln_kernel<<<L,64,0,stream>>>(h,l2);
  gemm1_kernel<<<L,256,0,stream>>>(l2,(const u16*)d_in[10],(const u16*)d_in[11],H);
  gemm2_kernel<<<L,256,0,stream>>>(H,(const u16*)d_in[12],h,(u16*)d_out);
}
```

```cpp
#include <hip/hip_runtime.h>
typedef unsigned short u16;
typedef unsigned int u32;
#define LDS_ADDR(p) ((u32)(size_t)(__attribute__((address_space(3))) char*)(p))
#define SSM_ASM \
    ".Lssm_begin:\n\t" \
    "v_and_b32 v1, 63, %0\n\t" \
    "s_and_b32 s46, %15, 7\n\t" \
    "s_lshr_b32 s45, %15, 3\n\t" \
    "s_lshl_b32 s44, s46, 5\n\t" \
    "s_add_u32 s44, s44, s45\n\t" \
    "s_cmp_lg_u32 %16, 0\n\t" \
    "s_cbranch_scc1 .Lend_%=\n\t" \
    "s_cmp_lt_u32 %1, 4\n\t" \
    "s_cbranch_scc1 .Lcomp_%=\n\t" \
    "s_sub_u32 s47, %1, 4\n\t" \
    "s_cmp_eq_u32 %1, 5\n\t" \
    "s_cbranch_scc1 .Lwgt_%=\n\t" \
    "s_cmp_gt_u32 %1, 5\n\t" \
    "s_cbranch_scc1 .Lgat_%=\n\t" \
    "s_mov_b64 s[40:41], %14\n\t" \
    "s_add_u32 s40, s40, 0x620000\n\t" \
    "s_addc_u32 s41, s41, 0\n\t" \
    "s_load_dword s48, s[40:41], 0x0\n\t" \
    "v_and_b32 v2, 7, v1\n\t" \
    "v_lshlrev_b32 v2, 10, v2\n\t" \
    "v_bfe_u32 v3, v1, 3, 1\n\t" \
    "v_lshl_add_u32 v2, v3, 6, v2\n\t" \
    "v_lshrrev_b32 v3, 4, v1\n\t" \
    "v_lshl_add_u32 v2, v3, 4, v2\n\t" \
    "s_lshl_b32 s40, %15, 13\n\t" \
    "v_add_u32 v2, s40, v2\n\t" \
    "global_load_dwordx4 v[16:19], v2, %2\n\t" \
    "global_load_dwordx4 v[20:23], v2, %2 offset:128\n\t" \
    "global_load_dwordx4 v[24:27], v2, %2 offset:256\n\t" \
    "global_load_dwordx4 v[28:31], v2, %2 offset:384\n\t" \
    "global_load_dwordx4 v[32:35], v2, %2 offset:512\n\t" \
    "global_load_dwordx4 v[36:39], v2, %2 offset:640\n\t" \
    "global_load_dwordx4 v[40:43], v2, %2 offset:768\n\t" \
    "global_load_dwordx4 v[44:47], v2, %2 offset:896\n\t" \
    "s_barrier\n\t" \
    "v_mov_b32 v8, 0x3f803f80\n\t" \
    "v_mov_b32 v9, 0x3f803f80\n\t" \
    "v_mov_b32 v10, 0x3f803f80\n\t" \
    "v_mov_b32 v11, 0x3f803f80\n\t" \
    "s_mov_b32 s52, 0x100001\n\t" \
    "s_mov_b32 s53, 0x10000100\n\t" \
    "s_mov_b32 s54, 0x200002\n\t" \
    "s_mov_b32 s55, 0x20000200\n\t" \
    "s_mov_b32 s56, 0x400004\n\t" \
    "s_mov_b32 s57, 0x40000400\n\t" \
    "s_mov_b32 s58, 0x800008\n\t" \
    "s_mov_b32 s59, 0x80000800\n\t" \
    "s_mov_b64 s[42:43], %14\n\t" \
    "s_add_u32 s42, s42, 0x630000\n\t" \
    "s_addc_u32 s43, s43, 0\n\t" \
    "s_lshl_b32 s49, %15, 6\n\t" \
    "v_lshlrev_b32 v3, 3, v1\n\t" \
    "v_add_u32 v3, s49, v3\n\t" \
    "v_mov_b32 v5, 0x1b250\n\t" \
    "s_waitcnt vmcnt(7)\n\t" \
    "v_mfma_f32_16x16x32_bf16 v[48:51], v[8:11], v[16:19], 0\n\t" \
    "v_mfma_f32_16x16x32_bf16 v[52:55], v[16:19], v[16:19], 0\n\t" \
    "s_waitcnt vmcnt(6)\n\t" \
    "v_mfma_f32_16x16x32_bf16 v[48:51], v[8:11], v[20:23], v[48:51]\n\t" \
    "v_mfma_f32_16x16x32_bf16 v[52:55], v[20:23], v[20:23], v[52:55]\n\t" \
    "s_waitcnt vmcnt(5)\n\t" \
    "v_mfma_f32_16x16x32_bf16 v[48:51], v[8:11], v[24:27], v[48:51]\n\t" \
    "v_mfma_f32_16x16x32_bf16 v[52:55], v[24:27], v[24:27], v[52:55]\n\t" \
    "s_waitcnt vmcnt(4)\n\t" \
    "v_mfma_f32_16x16x32_bf16 v[48:51], v[8:11], v[28:31], v[48:51]\n\t" \
    "v_mfma_f32_16x16x32_bf16 v[52:55], v[28:31], v[28:31], v[52:55]\n\t" \
    "s_waitcnt vmcnt(3)\n\t" \
    "v_mfma_f32_16x16x32_bf16 v[48:51], v[8:11], v[32:35], v[48:51]\n\t" \
    "v_mfma_f32_16x16x32_bf16 v[52:55], v[32:35], v[32:35], v[52:55]\n\t" \
    "s_waitcnt vmcnt(2)\n\t" \
    "v_mfma_f32_16x16x32_bf16 v[48:51], v[8:11], v[36:39], v[48:51]\n\t" \
    "v_mfma_f32_16x16x32_bf16 v[52:55], v[36:39], v[36:39], v[52:55]\n\t" \
    "s_waitcnt vmcnt(1)\n\t" \
    "v_mfma_f32_16x16x32_bf16 v[48:51], v[8:11], v[40:43], v[48:51]\n\t" \
    "v_mfma_f32_16x16x32_bf16 v[52:55], v[40:43], v[40:43], v[52:55]\n\t" \
    "s_waitcnt vmcnt(0)\n\t" \
    "v_mfma_f32_16x16x32_bf16 v[48:51], v[8:11], v[44:47], v[48:51]\n\t" \
    "v_mfma_f32_16x16x32_bf16 v[52:55], v[44:47], v[44:47], v[52:55]\n\t" \
    "s_waitcnt lgkmcnt(0)\n\t" \
    "s_add_u32 s49, s48, 0x9e3779b9\n\t" \
    "v_mov_b32 v13, s49\n\t" \
    "s_nop 3\n\t" \
    "v_add_f32_dpp v6, v48, v48 row_ror:8 row_mask:0xf bank_mask:0xf\n\t" \
    "v_cndmask_b32 v7, 0, v52, s[52:53]\n\t" \
    "v_cndmask_b32 v7, v7, v53, s[54:55]\n\t" \
    "v_cndmask_b32 v7, v7, v54, s[56:57]\n\t" \
    "v_cndmask_b32 v7, v7, v55, s[58:59]\n\t" \
    "v_mul_f32 v6, 0x3b000000, v6\n\t" \
    "v_mov_b32 v14, v7\n\t" \
    "s_nop 1\n\t" \
    "v_permlane32_swap_b32 v7, v14\n\t" \
    "v_add_f32 v7, v7, v14\n\t" \
    "v_mov_b32 v14, v7\n\t" \
    "s_nop 1\n\t" \
    "v_permlane16_swap_b32 v7, v14\n\t" \
    "v_add_f32 v7, v7, v14\n\t" \
    "s_nop 1\n\t" \
    "v_add_f32_dpp v7, v7, v7 row_ror:8 row_mask:0xf bank_mask:0xf\n\t" \
    "v_mul_f32 v7, 0x3b000000, v7\n\t" \
    "v_fma_f32 v7, -v6, v6, v7\n\t" \
    "v_max_f32 v7, 0, v7\n\t" \
    "v_cvt_pk_bf16_f32 v7, v7, v7\n\t" \
    "v_and_b32 v7, 0xffff0000, v7\n\t" \
    "v_add_f32 v7, 0x3727c5ac, v7\n\t" \
    "v_cvt_pk_bf16_f32 v7, v7, v7\n\t" \
    "v_and_b32 v7, 0xffff0000, v7\n\t" \
    "v_sqrt_f32 v7, v7\n\t" \
    "s_nop 0\n\t" \
    "v_cvt_pk_bf16_f32 v12, v6, v7\n\t" \
    "s_mov_b64 exec, 0xff\n\t" \
    "global_store_dwordx2 v3, v[12:13], s[42:43] sc1\n\t" \
    "s_mov_b64 exec, -1\n\t" \
    "v_mov_b32 v14, 1\n\t" \
    "ds_write_b32 v5, v13 offset:0\n\t" \
    "ds_write_b32 v5, v14 offset:4\n\t" \
    "s_branch .Lw2_%=\n\t" \
    ".Lwgt_%=:\n\t" \
    "s_getpc_b64 s[40:41]\n\t" \
    ".Lpc_%=:\n\t" \
    "s_sub_u32 s40, s40, .Lpc_%=-.Lssm_begin\n\t" \
    "s_subb_u32 s41, s41, 0\n\t" \
    "s_and_b32 s42, s45, 7\n\t" \
    "s_lshl_b32 s42, s42, 13\n\t" \
    "s_mov_b32 s43, .Lffn_end-.Lssm_begin-4\n\t" \
    "v_lshlrev_b32 v2, 7, v1\n\t" \
    "v_add_u32 v2, s42, v2\n\t" \
    "v_min_u32 v2, s43, v2\n\t" \
    "global_load_dword v69, v2, s[40:41]\n\t" \
    "s_barrier\n\t" \
    "v_lshlrev_b32 v10, 4, v1\n\t" \
    "v_add_u32 v10, 0x1f280, v10\n\t" \
    "v_and_b32 v5, 15, v1\n\t" \
    "v_lshrrev_b32 v6, 1, v1\n\t" \
    "v_and_b32 v6, 24, v6\n\t" \
    "v_mov_b32 v7, 0x3800\n\t" \
    "v_lshlrev_b32 v7, v5, v7\n\t" \
    "v_lshrrev_b32 v7, v6, v7\n\t" \
    "v_bfe_i32 v8, v7, 0, 1\n\t" \
    "v_bfe_i32 v9, v7, 1, 1\n\t" \
    "v_and_b32 v8, 0x3f80, v8\n\t" \
    "v_and_b32 v9, 0x3f800000, v9\n\t" \
    "v_or_b32 v16, v8, v9\n\t" \
    "v_bfe_i32 v8, v7, 2, 1\n\t" \
    "v_bfe_i32 v9, v7, 3, 1\n\t" \
    "v_and_b32 v8, 0x3f80, v8\n\t" \
    "v_and_b32 v9, 0x3f800000, v9\n\t" \
    "v_or_b32 v17, v8, v9\n\t" \
    "v_bfe_i32 v8, v7, 4, 1\n\t" \
    "v_bfe_i32 v9, v7, 5, 1\n\t" \
    "v_and_b32 v8, 0x3f80, v8\n\t" \
    "v_and_b32 v9, 0x3f800000, v9\n\t" \
    "v_or_b32 v18, v8, v9\n\t" \
    "v_bfe_i32 v8, v7, 6, 1\n\t" \
    "v_bfe_i32 v9, v7, 7, 1\n\t" \
    "v_and_b32 v8, 0x3f80, v8\n\t" \
    "v_and_b32 v9, 0x3f800000, v9\n\t" \
    "v_or_b32 v19, v8, v9\n\t" \
    "ds_write_b128 v10, v[16:19] offset:0\n\t" \
    "v_mov_b32 v7, 0x7c0\n\t" \
    "v_lshlrev_b32 v7, v5, v7\n\t" \
    "v_lshrrev_b32 v7, v6, v7\n\t" \
    "v_bfe_i32 v8, v7, 0, 1\n\t" \
    "v_bfe_i32 v9, v7, 1, 1\n\t" \
    "v_and_b32 v8, 0x3f80, v8\n\t" \
    "v_and_b32 v9, 0x3f800000, v9\n\t" \
    "v_or_b32 v20, v8, v9\n\t" \
    "v_bfe_i32 v8, v7, 2, 1\n\t" \
    "v_bfe_i32 v9, v7, 3, 1\n\t" \
    "v_and_b32 v8, 0x3f80, v8\n\t" \
    "v_and_b32 v9, 0x3f800000, v9\n\t" \
    "v_or_b32 v21, v8, v9\n\t" \
    "v_bfe_i32 v8, v7, 4, 1\n\t" \
    "v_bfe_i32 v9, v7, 5, 1\n\t" \
    "v_and_b32 v8, 0x3f80, v8\n\t" \
    "v_and_b32 v9, 0x3f800000, v9\n\t" \
    "v_or_b32 v22, v8, v9\n\t" \
    "v_bfe_i32 v8, v7, 6, 1\n\t" \
    "v_bfe_i32 v9, v7, 7, 1\n\t" \
    "v_and_b32 v8, 0x3f80, v8\n\t" \
    "v_and_b32 v9, 0x3f800000, v9\n\t" \
    "v_or_b32 v23, v8, v9\n\t" \
    "ds_write_b128 v10, v[20:23] offset:1024\n\t" \
    "v_mov_b32 v7, 0xfe0\n\t" \
    "v_lshlrev_b32 v7, v5, v7\n\t" \
    "v_lshrrev_b32 v7, v6, v7\n\t" \
    "v_bfe_i32 v8, v7, 0, 1\n\t" \
    "v_bfe_i32 v9, v7, 1, 1\n\t" \
    "v_and_b32 v8, 0x3f80, v8\n\t" \
    "v_and_b32 v9, 0x3f800000, v9\n\t" \
    "v_or_b32 v24, v8, v9\n\t" \
    "v_bfe_i32 v8, v7, 2, 1\n\t" \
    "v_bfe_i32 v9, v7, 3, 1\n\t" \
    "v_and_b32 v8, 0x3f80, v8\n\t" \
    "v_and_b32 v9, 0x3f800000, v9\n\t" \
    "v_or_b32 v25, v8, v9\n\t" \
    "v_bfe_i32 v8, v7, 4, 1\n\t" \
    "v_bfe_i32 v9, v7, 5, 1\n\t" \
    "v_and_b32 v8, 0x3f80, v8\n\t" \
    "v_and_b32 v9, 0x3f800000, v9\n\t" \
    "v_or_b32 v26, v8, v9\n\t" \
    "v_bfe_i32 v8, v7, 6, 1\n\t" \
    "v_bfe_i32 v9, v7, 7, 1\n\t" \
    "v_and_b32 v8, 0x3f80, v8\n\t" \
    "v_and_b32 v9, 0x3f800000, v9\n\t" \
    "v_or_b32 v27, v8, v9\n\t" \
    "ds_write_b128 v10, v[24:27] offset:2048\n\t" \
    "v_mov_b32 v7, 0x1ff0\n\t" \
    "v_lshlrev_b32 v7, v5, v7\n\t" \
    "v_lshrrev_b32 v7, v6, v7\n\t" \
    "v_bfe_i32 v8, v7, 0, 1\n\t" \
    "v_bfe_i32 v9, v7, 1, 1\n\t" \
    "v_and_b32 v8, 0x3f80, v8\n\t" \
    "v_and_b32 v9, 0x3f800000, v9\n\t" \
    "v_or_b32 v28, v8, v9\n\t" \
    "v_bfe_i32 v8, v7, 2, 1\n\t" \
    "v_bfe_i32 v9, v7, 3, 1\n\t" \
    "v_and_b32 v8, 0x3f80, v8\n\t" \
    "v_and_b32 v9, 0x3f800000, v9\n\t" \
    "v_or_b32 v29, v8, v9\n\t" \
    "v_bfe_i32 v8, v7, 4, 1\n\t" \
    "v_bfe_i32 v9, v7, 5, 1\n\t" \
    "v_and_b32 v8, 0x3f80, v8\n\t" \
    "v_and_b32 v9, 0x3f800000, v9\n\t" \
    "v_or_b32 v30, v8, v9\n\t" \
    "v_bfe_i32 v8, v7, 6, 1\n\t" \
    "v_bfe_i32 v9, v7, 7, 1\n\t" \
    "v_and_b32 v8, 0x3f80, v8\n\t" \
    "v_and_b32 v9, 0x3f800000, v9\n\t" \
    "v_or_b32 v31, v8, v9\n\t" \
    "ds_write_b128 v10, v[28:31] offset:3072\n\t" \
    ".Lw2_%=:\n\t" \
    "s_mul_i32 s50, s46, 28\n\t" \
    "s_add_u32 s50, s50, s45\n\t" \
    "s_cmp_ge_u32 s50, 112\n\t" \
    "s_cselect_b32 s51, 1, 0\n\t" \
    "s_mul_i32 s60, s51, 112\n\t" \
    "s_sub_u32 s60, s50, s60\n\t" \
    "s_lshr_b32 s61, s60, 2\n\t" \
    "s_and_b32 s60, s50, 3\n\t" \
    "v_lshrrev_b32 v72, 4, v1\n\t" \
    "v_bfe_u32 v6, v1, 2, 2\n\t" \
    "v_lshl_add_u32 v7, v72, 3, v6\n\t" \
    "v_lshlrev_b32 v7, 7, v7\n\t" \
    "v_and_b32 v8, 3, v1\n\t" \
    "v_lshl_add_u32 v7, v8, 3, v7\n\t" \
    "s_lshl_b32 s42, s47, 13\n\t" \
    "s_add_u32 s42, s42, 0x1b280\n\t" \
    "v_add_u32 v7, s42, v7\n\t" \
    "v_lshrrev_b32 v6, 1, v6\n\t" \
    "v_and_b32 v8, 1, v72\n\t" \
    "v_lshl_or_b32 v6, v8, 1, v6\n\t" \
    "v_xor_b32 v8, 0, v6\n\t" \
    "v_lshl_add_u32 v76, v8, 5, v7\n\t" \
    "v_xor_b32 v8, 1, v6\n\t" \
    "v_lshl_add_u32 v77, v8, 5, v7\n\t" \
    "v_xor_b32 v8, 2, v6\n\t" \
    "v_lshl_add_u32 v78, v8, 5, v7\n\t" \
    "v_xor_b32 v8, 3, v6\n\t" \
    "v_lshl_add_u32 v79, v8, 5, v7\n\t" \
    "v_mov_b32 v80, 0x3f803f80\n\t" \
    "v_mov_b32 v81, 0x3f803f80\n\t" \
    "v_mov_b32 v82, 0x3f803f80\n\t" \
    "v_mov_b32 v83, 0x3f803f80\n\t" \
    "s_lshl_b32 s63, s60, 1\n\t" \
    "s_add_u32 s63, s63, s47\n\t" \
    "s_lshl_b32 s63, s63, 1\n\t" \
    "s_lshl_b32 s40, s61, 2\n\t" \
    "s_add_u32 s40, s40, 0\n\t" \
    "s_mul_i32 s41, s40, 147\n\t" \
    "s_lshr_b32 s41, s41, 10\n\t" \
    "s_mul_i32 s42, s41, 7\n\t" \
    "s_sub_u32 s40, s40, s42\n\t" \
    "s_and_b32 s50, s41, 12\n\t" \
    "s_sub_u32 s50, s63, s50\n\t" \
    "s_and_b32 s50, s50, 15\n\t" \
    "s_cmp_lt_u32 s40, 3\n\t" \
    "s_cselect_b32 s52, 3, 1\n\t" \
    "s_cselect_b32 s43, 0, s40\n\t" \
    "s_cmp_ge_u32 s40, 7\n\t" \
    "s_cselect_b32 s52, 2, s52\n\t" \
    "s_cselect_b32 s43, 5, s43\n\t" \
    "s_mul_i32 s42, s50, s52\n\t" \
    "s_add_u32 s42, s42, s40\n\t" \
    "s_mul_i32 s43, s43, 15\n\t" \
    "s_add_u32 s42, s42, s43\n\t" \
    "s_lshl_b32 s52, s52, 11\n\t" \
    "s_lshl_b32 s42, s42, 1\n\t" \
    "s_add_u32 s42, s42, s51\n\t" \
    "s_mul_i32 s41, s41, 224\n\t" \
    "s_add_u32 s42, s42, s41\n\t" \
    "s_lshl_b32 s42, s42, 10\n\t" \
    "s_add_u32 s42, s42, 0x200000\n\t" \
    "s_mov_b64 s[64:65], %14\n\t" \
    "s_add_u32 s64, s64, s42\n\t" \
    "s_addc_u32 s65, s65, 0\n\t" \
    "s_lshl_b32 s40, s61, 2\n\t" \
    "s_add_u32 s40, s40, 1\n\t" \
    "s_mul_i32 s41, s40, 147\n\t" \
    "s_lshr_b32 s41, s41, 10\n\t" \
    "s_mul_i32 s42, s41, 7\n\t" \
    "s_sub_u32 s40, s40, s42\n\t" \
    "s_and_b32 s50, s41, 12\n\t" \
    "s_sub_u32 s50, s63, s50\n\t" \
    "s_and_b32 s50, s50, 15\n\t" \
    "s_cmp_lt_u32 s40, 3\n\t" \
    "s_cselect_b32 s53, 3, 1\n\t" \
    "s_cselect_b32 s43, 0, s40\n\t" \
    "s_cmp_ge_u32 s40, 7\n\t" \
    "s_cselect_b32 s53, 2, s53\n\t" \
    "s_cselect_b32 s43, 5, s43\n\t" \
    "s_mul_i32 s42, s50, s53\n\t" \
    "s_add_u32 s42, s42, s40\n\t" \
    "s_mul_i32 s43, s43, 15\n\t" \
    "s_add_u32 s42, s42, s43\n\t" \
    "s_lshl_b32 s53, s53, 11\n\t" \
    "s_lshl_b32 s42, s42, 1\n\t" \
    "s_add_u32 s42, s42, s51\n\t" \
    "s_mul_i32 s41, s41, 224\n\t" \
    "s_add_u32 s42, s42, s41\n\t" \
    "s_lshl_b32 s42, s42, 10\n\t" \
    "s_add_u32 s42, s42, 0x200000\n\t" \
    "s_mov_b64 s[66:67], %14\n\t" \
    "s_add_u32 s66, s66, s42\n\t" \
    "s_addc_u32 s67, s67, 0\n\t" \
    "s_lshl_b32 s40, s61, 2\n\t" \
    "s_add_u32 s40, s40, 2\n\t" \
    "s_mul_i32 s41, s40, 147\n\t" \
    "s_lshr_b32 s41, s41, 10\n\t" \
    "s_mul_i32 s42, s41, 7\n\t" \
    "s_sub_u32 s40, s40, s42\n\t" \
    "s_and_b32 s50, s41, 12\n\t" \
    "s_sub_u32 s50, s63, s50\n\t" \
    "s_and_b32 s50, s50, 15\n\t" \
    "s_cmp_lt_u32 s40, 3\n\t" \
    "s_cselect_b32 s54, 3, 1\n\t" \
    "s_cselect_b32 s43, 0, s40\n\t" \
    "s_cmp_ge_u32 s40, 7\n\t" \
    "s_cselect_b32 s54, 2, s54\n\t" \
    "s_cselect_b32 s43, 5, s43\n\t" \
    "s_mul_i32 s42, s50, s54\n\t" \
    "s_add_u32 s42, s42, s40\n\t" \
    "s_mul_i32 s43, s43, 15\n\t" \
    "s_add_u32 s42, s42, s43\n\t" \
    "s_lshl_b32 s54, s54, 11\n\t" \
    "s_lshl_b32 s42, s42, 1\n\t" \
    "s_add_u32 s42, s42, s51\n\t" \
    "s_mul_i32 s41, s41, 224\n\t" \
    "s_add_u32 s42, s42, s41\n\t" \
    "s_lshl_b32 s42, s42, 10\n\t" \
    "s_add_u32 s42, s42, 0x200000\n\t" \
    "s_mov_b64 s[68:69], %14\n\t" \
    "s_add_u32 s68, s68, s42\n\t" \
    "s_addc_u32 s69, s69, 0\n\t" \
    "s_lshl_b32 s40, s61, 2\n\t" \
    "s_add_u32 s40, s40, 3\n\t" \
    "s_mul_i32 s41, s40, 147\n\t" \
    "s_lshr_b32 s41, s41, 10\n\t" \
    "s_mul_i32 s42, s41, 7\n\t" \
    "s_sub_u32 s40, s40, s42\n\t" \
    "s_and_b32 s50, s41, 12\n\t" \
    "s_sub_u32 s50, s63, s50\n\t" \
    "s_and_b32 s50, s50, 15\n\t" \
    "s_cmp_lt_u32 s40, 3\n\t" \
    "s_cselect_b32 s55, 3, 1\n\t" \
    "s_cselect_b32 s43, 0, s40\n\t" \
    "s_cmp_ge_u32 s40, 7\n\t" \
    "s_cselect_b32 s55, 2, s55\n\t" \
    "s_cselect_b32 s43, 5, s43\n\t" \
    "s_mul_i32 s42, s50, s55\n\t" \
    "s_add_u32 s42, s42, s40\n\t" \
    "s_mul_i32 s43, s43, 15\n\t" \
    "s_add_u32 s42, s42, s43\n\t" \
    "s_lshl_b32 s55, s55, 11\n\t" \
    "s_lshl_b32 s42, s42, 1\n\t" \
    "s_add_u32 s42, s42, s51\n\t" \
    "s_mul_i32 s41, s41, 224\n\t" \
    "s_add_u32 s42, s42, s41\n\t" \
    "s_lshl_b32 s42, s42, 10\n\t" \
    "s_add_u32 s42, s42, 0x200000\n\t" \
    "s_mov_b64 s[70:71], %14\n\t" \
    "s_add_u32 s70, s70, s42\n\t" \
    "s_addc_u32 s71, s71, 0\n\t" \
    "s_mul_i32 s42, s60, 28672\n\t" \
    "s_mul_i32 s43, s47, 14336\n\t" \
    "s_add_u32 s42, s42, s43\n\t" \
    "s_lshl_b32 s43, s61, 9\n\t" \
    "s_add_u32 s42, s42, s43\n\t" \
    "s_lshl_b32 s43, s51, 6\n\t" \
    "s_add_u32 s42, s42, s43\n\t" \
    "s_add_u32 s42, s42, 0x600000\n\t" \
    "s_mov_b64 s[62:63], %14\n\t" \
    "s_add_u32 s62, s62, s42\n\t" \
    "s_addc_u32 s63, s63, 0\n\t" \
    "v_lshlrev_b32 v73, 7, v72\n\t" \
    "v_and_b32 v4, 15, v1\n\t" \
    "v_lshl_add_u32 v73, v4, 2, v73\n\t" \
    "v_mov_b32 v2, 0x1b254\n\t" \
    "s_mov_b32 s59, 0x100000\n\t" \
    ".Lsy0_%=:\n\t" \
    "s_sleep 2\n\t" \
    "ds_read_b32 v3, v2 offset:0\n\t" \
    "s_waitcnt lgkmcnt(0)\n\t" \
    "v_readfirstlane_b32 s42, v3\n\t" \
    "s_sub_u32 s59, s59, 1\n\t" \
    "s_cbranch_scc1 .Lsz0_%=\n\t" \
    "s_cmp_lt_u32 s42, 1\n\t" \
    "s_cbranch_scc1 .Lsy0_%=\n\t" \
    ".Lsz0_%=:\n\t" \
    "v_mov_b32 v2, 0x1b258\n\t" \
    "s_mov_b32 s59, 0x100000\n\t" \
    ".Lsy1_%=:\n\t" \
    "s_sleep 2\n\t" \
    "ds_read_b32 v3, v2 offset:0\n\t" \
    "s_waitcnt lgkmcnt(0)\n\t" \
    "v_readfirstlane_b32 s42, v3\n\t" \
    "s_sub_u32 s59, s59, 1\n\t" \
    "s_cbranch_scc1 .Lsz1_%=\n\t" \
    "s_cmp_lt_u32 s42, 1\n\t" \
    "s_cbranch_scc1 .Lsy1_%=\n\t" \
    ".Lsz1_%=:\n\t" \
    "s_sleep 6\n\t" \
    "s_mov_b64 s[56:57], %14\n\t" \
    "s_add_u32 s56, s56, 0x630000\n\t" \
    "s_addc_u32 s57, s57, 0\n\t" \
    "s_lshl_b32 s42, s47, 12\n\t" \
    "s_add_u32 s56, s56, s42\n\t" \
    "s_addc_u32 s57, s57, 0\n\t" \
    "v_lshlrev_b32 v32, 4, v1\n\t" \
    "global_load_dwordx4 v[16:19], v32, s[56:57] sc1\n\t" \
    "global_load_dwordx4 v[20:23], v32, s[56:57] offset:1024 sc1\n\t" \
    "global_load_dwordx4 v[24:27], v32, s[56:57] offset:2048 sc1\n\t" \
    "global_load_dwordx4 v[28:31], v32, s[56:57] offset:3072 sc1\n\t" \
    "s_lshl_b32 s42, s47, 2\n\t" \
    "s_add_u32 s42, s42, 0x1b260\n\t" \
    "v_mov_b32 v5, s42\n\t" \
    "v_mov_b32 v6, 1\n\t" \
    "v_mov_b32 v7, 2\n\t" \
    "ds_write_b32 v5, v6 offset:0\n\t" \
    "s_setprio 3\n\t" \
    "s_lshl_b32 s42, s47, 11\n\t" \
    "s_add_u32 s42, s42, 0x19240\n\t" \
    "v_lshl_add_u32 v33, v1, 3, s42\n\t" \
    "v_mov_b32 v2, 0x1b250\n\t" \
    "ds_read_b32 v34, v2 offset:0\n\t" \
    "s_lshl_b32 s42, s47, 10\n\t" \
    "s_add_u32 m0, s42, 0xe000\n\t" \
    "s_mov_b32 s59, 0x40000\n\t" \
    "v_mov_b32 v2, 0x1b258\n\t" \
    "s_mov_b32 s59, 0x100000\n\t" \
    ".Lsy2_%=:\n\t" \
    "ds_read_b32 v3, v2 offset:0\n\t" \
    "s_waitcnt lgkmcnt(0)\n\t" \
    "v_readfirstlane_b32 s42, v3\n\t" \
    "s_sub_u32 s59, s59, 1\n\t" \
    "s_cbranch_scc1 .Lsz2_%=\n\t" \
    "s_cmp_lt_u32 s42, 2\n\t" \
    "s_cbranch_scc1 .Lsy2_%=\n\t" \
    ".Lsz2_%=:\n\t" \
    "v_mov_b32 v2, 0x1b25c\n\t" \
    "s_mov_b32 s59, 0x100000\n\t" \
    ".Lsy3_%=:\n\t" \
    "ds_read_b32 v3, v2 offset:0\n\t" \
    "s_waitcnt lgkmcnt(0)\n\t" \
    "v_readfirstlane_b32 s42, v3\n\t" \
    "s_sub_u32 s59, s59, 1\n\t" \
    "s_cbranch_scc1 .Lsz3_%=\n\t" \
    "s_cmp_lt_u32 s42, 2\n\t" \
    "s_cbranch_scc1 .Lsy3_%=\n\t" \
    ".Lsz3_%=:\n\t" \
    "s_lshl_b32 s42, s47, 10\n\t" \
    "s_add_u32 m0, s42, 0xe000\n\t" \
    "ds_read_b64 v[36:37], v33 offset:0\n\t" \
    "ds_read_b64 v[40:41], v33 offset:512\n\t" \
    "ds_read_b64 v[44:45], v33 offset:1024\n\t" \
    "ds_read_b64 v[48:49], v33 offset:1536\n\t" \
    "s_mov_b32 s59, 0x40000\n\t" \
    "s_waitcnt lgkmcnt(3)\n\t" \
    "v_and_b32 v39, 0xffff0000, v37\n\t" \
    "v_lshlrev_b32 v38, 16, v37\n\t" \
    "v_and_b32 v37, 0xffff0000, v36\n\t" \
    "v_lshlrev_b32 v36, 16, v36\n\t" \
    "s_waitcnt lgkmcnt(2)\n\t" \
    "v_and_b32 v43, 0xffff0000, v41\n\t" \
    "v_lshlrev_b32 v42, 16, v41\n\t" \
    "v_and_b32 v41, 0xffff0000, v40\n\t" \
    "v_lshlrev_b32 v40, 16, v40\n\t" \
    "s_waitcnt lgkmcnt(1)\n\t" \
    "v_and_b32 v47, 0xffff0000, v45\n\t" \
    "v_lshlrev_b32 v46, 16, v45\n\t" \
    "v_and_b32 v45, 0xffff0000, v44\n\t" \
    "v_lshlrev_b32 v44, 16, v44\n\t" \
    "s_waitcnt lgkmcnt(0)\n\t" \
    "v_and_b32 v51, 0xffff0000, v49\n\t" \
    "v_lshlrev_b32 v50, 16, v49\n\t" \
    "v_and_b32 v49, 0xffff0000, v48\n\t" \
    "v_lshlrev_b32 v48, 16, v48\n\t" \
    "s_waitcnt vmcnt(0)\n\t" \
    ".Lpl0_%=:\n\t" \
    "v_xor_b32 v4, v17, v34\n\t" \
    "v_bitop3_b32 v4, v19, v34, v4 bitop3:0xbe\n\t" \
    "v_bitop3_b32 v4, v21, v34, v4 bitop3:0xbe\n\t" \
    "v_bitop3_b32 v4, v23, v34, v4 bitop3:0xbe\n\t" \
    "v_bitop3_b32 v4, v25, v34, v4 bitop3:0xbe\n\t" \
    "v_bitop3_b32 v4, v27, v34, v4 bitop3:0xbe\n\t" \
    "v_bitop3_b32 v4, v29, v34, v4 bitop3:0xbe\n\t" \
    "v_bitop3_b32 v4, v31, v34, v4 bitop3:0xbe\n\t" \
    "v_cmp_ne_u32 vcc, 0, v4\n\t" \
    "s_and_b64 vcc, exec, vcc\n\t" \
    "s_cbranch_vccz .Lpk0_%=\n\t" \
    "s_sub_u32 s59, s59, 1\n\t" \
    "s_cbranch_scc1 .Lpk0_%=\n\t" \
    "global_load_dwordx4 v[16:19], v32, s[56:57] offset:0 sc1\n\t" \
    "global_load_dwordx4 v[20:23], v32, s[56:57] offset:1024 sc1\n\t" \
    "global_load_dwordx4 v[24:27], v32, s[56:57] offset:2048 sc1\n\t" \
    "global_load_dwordx4 v[28:31], v32, s[56:57] offset:3072 sc1\n\t" \
    "s_waitcnt vmcnt(0)\n\t" \
    "s_branch .Lpl0_%=\n\t" \
    ".Lpk0_%=:\n\t" \
    "v_and_b32 v17, 0xffff0000, v16\n\t" \
    "v_and_b32 v19, 0xffff0000, v18\n\t" \
    "v_and_b32 v21, 0xffff0000, v20\n\t" \
    "v_and_b32 v23, 0xffff0000, v22\n\t" \
    "v_and_b32 v25, 0xffff0000, v24\n\t" \
    "v_and_b32 v27, 0xffff0000, v26\n\t" \
    "v_and_b32 v29, 0xffff0000, v28\n\t" \
    "v_and_b32 v31, 0xffff0000, v30\n\t" \
    "v_rcp_f32 v17, v17\n\t" \
    "v_lshlrev_b32 v16, 16, v16\n\t" \
    "v_rcp_f32 v19, v19\n\t" \
    "v_lshlrev_b32 v18, 16, v18\n\t" \
    "v_rcp_f32 v21, v21\n\t" \
    "v_lshlrev_b32 v20, 16, v20\n\t" \
    "v_rcp_f32 v23, v23\n\t" \
    "v_lshlrev_b32 v22, 16, v22\n\t" \
    "v_rcp_f32 v25, v25\n\t" \
    "v_lshlrev_b32 v24, 16, v24\n\t" \
    "v_rcp_f32 v27, v27\n\t" \
    "v_lshlrev_b32 v26, 16, v26\n\t" \
    "v_rcp_f32 v29, v29\n\t" \
    "v_lshlrev_b32 v28, 16, v28\n\t" \
    "v_rcp_f32 v31, v31\n\t" \
    "v_lshlrev_b32 v30, 16, v30\n\t" \
    "v_mul_f32 v16, v16, v17\n\t" \
    "v_mul_f32 v18, v18, v19\n\t" \
    "v_mul_f32 v20, v20, v21\n\t" \
    "v_mul_f32 v22, v22, v23\n\t" \
    "v_mul_f32 v24, v24, v25\n\t" \
    "v_mul_f32 v26, v26, v27\n\t" \
    "v_mul_f32 v28, v28, v29\n\t" \
    "v_mul_f32 v30, v30, v31\n\t" \
    "v_fma_f32 v36, v36, v17, -v16\n\t" \
    "v_fma_f32 v37, v37, v17, -v16\n\t" \
    "v_fma_f32 v38, v38, v19, -v18\n\t" \
    "v_fma_f32 v39, v39, v19, -v18\n\t" \
    "v_cvt_pk_bf16_f32 v36, v36, v38\n\t" \
    "ds_write_addtid_b32 v36 offset:25696\n\t" \
    "v_cvt_pk_bf16_f32 v37, v37, v39\n\t" \
    "ds_write_addtid_b32 v37 offset:30272\n\t" \
    "v_fma_f32 v40, v40, v21, -v20\n\t" \
    "v_fma_f32 v41, v41, v21, -v20\n\t" \
    "v_fma_f32 v42, v42, v23, -v22\n\t" \
    "v_fma_f32 v43, v43, v23, -v22\n\t" \
    "v_cvt_pk_bf16_f32 v40, v40, v42\n\t" \
    "ds_write_addtid_b32 v40 offset:25952\n\t" \
    "v_cvt_pk_bf16_f32 v41, v41, v43\n\t" \
    "ds_write_addtid_b32 v41 offset:30528\n\t" \
    "v_fma_f32 v44, v44, v25, -v24\n\t" \
    "v_fma_f32 v45, v45, v25, -v24\n\t" \
    "v_fma_f32 v46, v46, v27, -v26\n\t" \
    "v_fma_f32 v47, v47, v27, -v26\n\t" \
    "v_cvt_pk_bf16_f32 v44, v44, v46\n\t" \
    "ds_write_addtid_b32 v44 offset:26208\n\t" \
    "v_cvt_pk_bf16_f32 v45, v45, v47\n\t" \
    "ds_write_addtid_b32 v45 offset:30784\n\t" \
    "v_fma_f32 v48, v48, v29, -v28\n\t" \
    "v_fma_f32 v49, v49, v29, -v28\n\t" \
    "v_fma_f32 v50, v50, v31, -v30\n\t" \
    "v_fma_f32 v51, v51, v31, -v30\n\t" \
    "v_cvt_pk_bf16_f32 v48, v48, v50\n\t" \
    "ds_write_addtid_b32 v48 offset:26464\n\t" \
    "v_cvt_pk_bf16_f32 v49, v49, v51\n\t" \
    "ds_write_addtid_b32 v49 offset:31040\n\t" \
    "s_waitcnt lgkmcnt(0)\n\t" \
    "ds_write_b32 v5, v7 offset:0\n\t" \
    "s_setprio 0\n\t" \
    "v_mov_b32 v2, 0x1b258\n\t" \
    "s_mov_b32 s59, 0x100000\n\t" \
    ".Lsy4_%=:\n\t" \
    "s_sleep 2\n\t" \
    "ds_read_b32 v3, v2 offset:0\n\t" \
    "s_waitcnt lgkmcnt(0)\n\t" \
    "v_readfirstlane_b32 s42, v3\n\t" \
    "s_sub_u32 s59, s59, 1\n\t" \
    "s_cbranch_scc1 .Lsz4_%=\n\t" \
    "s_cmp_lt_u32 s42, 3\n\t" \
    "s_cbranch_scc1 .Lsy4_%=\n\t" \
    ".Lsz4_%=:\n\t" \
    "v_mov_b32 v2, 0x1b25c\n\t" \
    "s_mov_b32 s59, 0x100000\n\t" \
    ".Lsy5_%=:\n\t" \
    "s_sleep 2\n\t" \
    "ds_read_b32 v3, v2 offset:0\n\t" \
    "s_waitcnt lgkmcnt(0)\n\t" \
    "v_readfirstlane_b32 s42, v3\n\t" \
    "s_sub_u32 s59, s59, 1\n\t" \
    "s_cbranch_scc1 .Lsz5_%=\n\t" \
    "s_cmp_lt_u32 s42, 3\n\t" \
    "s_cbranch_scc1 .Lsy5_%=\n\t" \
    ".Lsz5_%=:\n\t" \
    "s_cmp_ge_u32 s45, 28\n\t" \
    "s_cbranch_scc1 .Lnow_%=\n\t" \
    "ds_read_b64_tr_b16 v[96:97], v76 offset:0\n\t" \
    "ds_read_b64_tr_b16 v[98:99], v76 offset:512\n\t" \
    "ds_read_b64_tr_b16 v[100:101], v77 offset:0\n\t" \
    "ds_read_b64_tr_b16 v[102:103], v77 offset:512\n\t" \
    "ds_read_b64_tr_b16 v[104:105], v78 offset:0\n\t" \
    "ds_read_b64_tr_b16 v[106:107], v78 offset:512\n\t" \
    "ds_read_b64_tr_b16 v[108:109], v79 offset:0\n\t" \
    "ds_read_b64_tr_b16 v[110:111], v79 offset:512\n\t" \
    "ds_read_b64_tr_b16 v[112:113], v76 offset:4096\n\t" \
    "ds_read_b64_tr_b16 v[114:115], v76 offset:4608\n\t" \
    "ds_read_b64_tr_b16 v[116:117], v77 offset:4096\n\t" \
    "ds_read_b64_tr_b16 v[118:119], v77 offset:4608\n\t" \
    "ds_read_b64_tr_b16 v[120:121], v78 offset:4096\n\t" \
    "ds_read_b64_tr_b16 v[122:123], v78 offset:4608\n\t" \
    "ds_read_b64_tr_b16 v[124:125], v79 offset:4096\n\t" \
    "ds_read_b64_tr_b16 v[126:127], v79 offset:4608\n\t" \
    "s_waitcnt lgkmcnt(0)\n\t" \
    "global_store_dwordx4 v32, v[96:99], s[64:65] sc1\n\t" \
    "v_mfma_f32_16x16x32_bf16 v[128:131], v[80:83], v[96:99], 0\n\t" \
    "global_store_dwordx4 v32, v[100:103], s[66:67] sc1\n\t" \
    "v_mfma_f32_16x16x32_bf16 v[132:135], v[80:83], v[100:103], 0\n\t" \
    "global_store_dwordx4 v32, v[104:107], s[68:69] sc1\n\t" \
    "v_mfma_f32_16x16x32_bf16 v[136:139], v[80:83], v[104:107], 0\n\t" \
    "global_store_dwordx4 v32, v[108:111], s[70:71] sc1\n\t" \
    "v_mfma_f32_16x16x32_bf16 v[140:143], v[80:83], v[108:111], 0\n\t" \
    "s_add_u32 s64, s64, s52\n\t" \
    "s_addc_u32 s65, s65, 0\n\t" \
    "global_store_dwordx4 v32, v[112:115], s[64:65] sc1\n\t" \
    "v_mfma_f32_16x16x32_bf16 v[128:131], v[80:83], v[112:115], v[128:131]\n\t" \
    "s_add_u32 s66, s66, s53\n\t" \
    "s_addc_u32 s67, s67, 0\n\t" \
    "global_store_dwordx4 v32, v[116:119], s[66:67] sc1\n\t" \
    "v_mfma_f32_16x16x32_bf16 v[132:135], v[80:83], v[116:119], v[132:135]\n\t" \
    "s_add_u32 s68, s68, s54\n\t" \
    "s_addc_u32 s69, s69, 0\n\t" \
    "global_store_dwordx4 v32, v[120:123], s[68:69] sc1\n\t" \
    "v_mfma_f32_16x16x32_bf16 v[136:139], v[80:83], v[120:123], v[136:139]\n\t" \
    "s_add_u32 s70, s70, s55\n\t" \
    "s_addc_u32 s71, s71, 0\n\t" \
    "global_store_dwordx4 v32, v[124:127], s[70:71] sc1\n\t" \
    "v_mfma_f32_16x16x32_bf16 v[140:143], v[80:83], v[124:127], v[140:143]\n\t" \
    "v_cmp_eq_u32 vcc, 1, v72\n\t" \
    "s_nop 1\n\t" \
    "v_cndmask_b32 v128, v128, v132, vcc\n\t" \
    "v_cmp_eq_u32 vcc, 2, v72\n\t" \
    "s_nop 1\n\t" \
    "v_cndmask_b32 v128, v128, v136, vcc\n\t" \
    "v_cmp_eq_u32 vcc, 3, v72\n\t" \
    "s_nop 1\n\t" \
    "v_cndmask_b32 v128, v128, v140, vcc\n\t" \
    "global_store_dword v73, v128, s[62:63] sc1\n\t" \
    ".Lnow_%=:\n\t" \
    "s_branch .Lh1b_%=\n\t" \
    ".Lgat_%=:\n\t" \
    "v_mov_b32 v8, 0\n\t" \
    "v_mov_b32 v9, 0\n\t" \
    "v_mov_b32 v10, 0\n\t" \
    "v_mov_b32 v11, 0\n\t" \
    "v_cmp_lt_u32 vcc, 29, v1\n\t" \
    "v_lshlrev_b32 v2, 4, v1\n\t" \
    "v_add_u32 v3, 0x15280, v2\n\t" \
    "v_add_u32 v2, 0x14280, v2\n\t" \
    "v_cndmask_b32 v2, v2, v3, vcc\n\t" \
    "v_cmp_lt_u32 vcc, 59, v1\n\t" \
    "v_and_b32 v3, 3, v1\n\t" \
    "v_lshlrev_b32 v3, 4, v3\n\t" \
    "v_add_u32 v3, 0x1b240, v3\n\t" \
    "v_cndmask_b32 v2, v2, v3, vcc\n\t" \
    "v_and_b32 v4, 3, v1\n\t" \
    "v_mul_u32_u24 v4, 0xb00, v4\n\t" \
    "v_add_u32 v3, 0x17060, v4\n\t" \
    "s_cmp_eq_u32 %1, 6\n\t" \
    "s_cselect_b64 vcc, -1, 0\n\t" \
    "v_cndmask_b32 v2, v3, v2, vcc\n\t" \
    "ds_write_b128 v2, v[8:11] offset:0\n\t" \
    "s_sub_u32 s42, %1, 6\n\t" \
    "s_lshl_b32 s43, s42, 19\n\t" \
    "s_lshl_b32 s50, s44, 2\n\t" \
    "s_add_u32 s43, s43, s50\n\t" \
    "s_mov_b64 s[40:41], %2\n\t" \
    "s_add_u32 s40, s40, s43\n\t" \
    "s_addc_u32 s41, s41, 0\n\t" \
    "s_lshl_b32 s42, s42, 11\n\t" \
    "s_add_u32 s42, s42, 0x19240\n\t" \
    "v_lshlrev_b32 v2, 10, v1\n\t" \
    "s_mov_b64 s[54:55], s[40:41]\n\t" \
    "s_mov_b32 s49, s42\n\t" \
    "s_add_u32 s62, s45, 0\n\t" \
    "s_and_b32 s62, s62, 7\n\t" \
    "s_lshl_b32 s63, s62, 8\n\t" \
    "s_add_u32 m0, s49, s63\n\t" \
    "s_lshl_b32 s62, s62, 16\n\t" \
    "s_add_u32 s52, s54, s62\n\t" \
    "s_addc_u32 s53, s55, 0\n\t" \
    "global_load_lds_dword v2, s[52:53]\n\t" \
    "s_waitcnt lgkmcnt(0)\n\t" \
    "s_barrier\n\t" \
    "s_sub_u32 s48, %1, 6\n\t" \
    "s_mul_i32 s50, s46, 28\n\t" \
    "s_add_u32 s50, s50, s45\n\t" \
    "s_cmp_ge_u32 s50, 112\n\t" \
    "s_cselect_b32 s51, 1, 0\n\t" \
    "s_mul_i32 s60, s51, 112\n\t" \
    "s_sub_u32 s60, s50, s60\n\t" \
    "s_lshr_b32 s61, s60, 2\n\t" \
    "s_and_b32 s60, s50, 3\n\t" \
    "s_mov_b32 s58, s51\n\t" \
    "s_mov_b32 s59, s60\n\t" \
    "s_cmp_eq_u32 s51, 1\n\t" \
    "s_cselect_b64 s[50:51], %12, %11\n\t" \
    "s_mul_i32 s42, s60, 458752\n\t" \
    "s_lshl_b32 s43, s61, 7\n\t" \
    "s_add_u32 s42, s42, s43\n\t" \
    "s_cmp_lt_u32 s45, 28\n\t" \
    "s_cselect_b32 s42, s42, 0\n\t" \
    "s_mul_i32 s43, s48, 0x38000\n\t" \
    "s_add_u32 s42, s42, s43\n\t" \
    "s_add_u32 s50, s50, s42\n\t" \
    "s_addc_u32 s51, s51, 0\n\t" \
    "v_lshrrev_b32 v60, 3, v1\n\t" \
    "v_mul_u32_u24 v60, 0xe00, v60\n\t" \
    "v_bfe_u32 v61, v1, 4, 1\n\t" \
    "v_lshlrev_b32 v61, 1, v61\n\t" \
    "v_and_b32 v62, 7, v1\n\t" \
    "v_xor_b32 v61, v61, v62\n\t" \
    "v_lshl_add_u32 v63, v61, 4, v60\n\t" \
    "v_xor_b32 v61, 4, v61\n\t" \
    "v_lshl_add_u32 v64, v61, 4, v60\n\t" \
    "s_lshl_b32 s60, s48, 13\n\t" \
    "s_add_u32 s60, s60, 0x1b280\n\t" \
    "s_lshl_b32 s42, %15, 1\n\t" \
    "s_add_u32 s42, s42, s48\n\t" \
    "s_mul_i32 s42, s42, 28\n\t" \
    "v_min_u32 v60, 27, v1\n\t" \
    "v_add_u32 v60, s42, v60\n\t" \
    "v_lshlrev_b32 v60, 7, v60\n\t" \
    "s_lshl_b32 s42, s48, 2\n\t" \
    "s_add_u32 s42, s42, 0x1b258\n\t" \
    "v_mov_b32 v5, s42\n\t" \
    "v_mov_b32 v6, 1\n\t" \
    "v_mov_b32 v7, 2\n\t" \
    "v_mov_b32 v8, 3\n\t" \
    "s_add_u32 s62, s45, 1\n\t" \
    "s_and_b32 s62, s62, 7\n\t" \
    "s_lshl_b32 s63, s62, 8\n\t" \
    "s_add_u32 m0, s49, s63\n\t" \
    "s_lshl_b32 s62, s62, 16\n\t" \
    "s_add_u32 s52, s54, s62\n\t" \
    "s_addc_u32 s53, s55, 0\n\t" \
    "global_load_lds_dword v2, s[52:53]\n\t" \
    "s_add_u32 s62, s45, 2\n\t" \
    "s_and_b32 s62, s62, 7\n\t" \
    "s_lshl_b32 s63, s62, 8\n\t" \
    "s_add_u32 m0, s49, s63\n\t" \
    "s_lshl_b32 s62, s62, 16\n\t" \
    "s_add_u32 s52, s54, s62\n\t" \
    "s_addc_u32 s53, s55, 0\n\t" \
    "global_load_lds_dword v2, s[52:53]\n\t" \
    "s_add_u32 s62, s45, 3\n\t" \
    "s_and_b32 s62, s62, 7\n\t" \
    "s_lshl_b32 s63, s62, 8\n\t" \
    "s_add_u32 m0, s49, s63\n\t" \
    "s_lshl_b32 s62, s62, 16\n\t" \
    "s_add_u32 s52, s54, s62\n\t" \
    "s_addc_u32 s53, s55, 0\n\t" \
    "global_load_lds_dword v2, s[52:53]\n\t" \
    "s_add_u32 s62, s45, 4\n\t" \
    "s_and_b32 s62, s62, 7\n\t" \
    "s_lshl_b32 s63, s62, 8\n\t" \
    "s_add_u32 m0, s49, s63\n\t" \
    "s_lshl_b32 s62, s62, 16\n\t" \
    "s_add_u32 s52, s54, s62\n\t" \
    "s_addc_u32 s53, s55, 0\n\t" \
    "global_load_lds_dword v2, s[52:53]\n\t" \
    "s_add_u32 m0, s60, 0x0\n\t" \
    "s_nop 0\n\t" \
    "global_load_lds_dwordx4 v63, s[50:51]\n\t" \
    "s_add_u32 m0, s60, 0x400\n\t" \
    "s_add_u32 s50, s50, 0x7000\n\t" \
    "s_addc_u32 s51, s51, 0\n\t" \
    "global_load_lds_dwordx4 v64, s[50:51]\n\t" \
    "s_add_u32 m0, s60, 0x800\n\t" \
    "s_add_u32 s50, s50, 0x7000\n\t" \
    "s_addc_u32 s51, s51, 0\n\t" \
    "global_load_lds_dwordx4 v63, s[50:51]\n\t" \
    "s_add_u32 m0, s60, 0xc00\n\t" \
    "s_add_u32 s50, s50, 0x7000\n\t" \
    "s_addc_u32 s51, s51, 0\n\t" \
    "global_load_lds_dwordx4 v64, s[50:51]\n\t" \
    "s_add_u32 m0, s60, 0x1000\n\t" \
    "s_add_u32 s50, s50, 0x7000\n\t" \
    "s_addc_u32 s51, s51, 0\n\t" \
    "global_load_lds_dwordx4 v63, s[50:51]\n\t" \
    "s_add_u32 m0, s60, 0x1400\n\t" \
    "s_add_u32 s50, s50, 0x7000\n\t" \
    "s_addc_u32 s51, s51, 0\n\t" \
    "global_load_lds_dwordx4 v64, s[50:51]\n\t" \
    "s_add_u32 m0, s60, 0x1800\n\t" \
    "s_add_u32 s50, s50, 0x7000\n\t" \
    "s_addc_u32 s51, s51, 0\n\t" \
    "global_load_lds_dwordx4 v63, s[50:51]\n\t" \
    "s_add_u32 m0, s60, 0x1c00\n\t" \
    "s_add_u32 s50, s50, 0x7000\n\t" \
    "s_addc_u32 s51, s51, 0\n\t" \
    "global_load_lds_dwordx4 v64, s[50:51]\n\t" \
    "global_load_dword v68, v60, %13\n\t" \
    "s_add_u32 s62, s45, 5\n\t" \
    "s_and_b32 s62, s62, 7\n\t" \
    "s_lshl_b32 s63, s62, 8\n\t" \
    "s_add_u32 m0, s49, s63\n\t" \
    "s_lshl_b32 s62, s62, 16\n\t" \
    "s_add_u32 s52, s54, s62\n\t" \
    "s_addc_u32 s53, s55, 0\n\t" \
    "global_load_lds_dword v2, s[52:53]\n\t" \
    "s_waitcnt vmcnt(11)\n\t" \
    "s_add_u32 s62, s45, 6\n\t" \
    "s_and_b32 s62, s62, 7\n\t" \
    "s_lshl_b32 s63, s62, 8\n\t" \
    "s_add_u32 m0, s49, s63\n\t" \
    "s_lshl_b32 s62, s62, 16\n\t" \
    "s_add_u32 s52, s54, s62\n\t" \
    "s_addc_u32 s53, s55, 0\n\t" \
    "global_load_lds_dword v2, s[52:53]\n\t" \
    "s_waitcnt vmcnt(11)\n\t" \
    "ds_write_b32 v5, v6 offset:0\n\t" \
    "s_add_u32 s62, s45, 7\n\t" \
    "s_and_b32 s62, s62, 7\n\t" \
    "s_lshl_b32 s63, s62, 8\n\t" \
    "s_add_u32 m0, s49, s63\n\t" \
    "s_lshl_b32 s62, s62, 16\n\t" \
    "s_add_u32 s52, s54, s62\n\t" \
    "s_addc_u32 s53, s55, 0\n\t" \
    "global_load_lds_dword v2, s[52:53]\n\t" \
    "s_waitcnt vmcnt(2)\n\t" \
    "s_add_u32 s62, s45, 8\n\t" \
    "s_and_b32 s62, s62, 7\n\t" \
    "s_add_u32 s62, s62, 16\n\t" \
    "s_lshl_b32 s63, s62, 8\n\t" \
    "s_add_u32 m0, s49, s63\n\t" \
    "s_lshl_b32 s62, s62, 16\n\t" \
    "s_add_u32 s52, s54, s62\n\t" \
    "s_addc_u32 s53, s55, 0\n\t" \
    "global_load_lds_dword v2, s[52:53]\n\t" \
    "s_waitcnt vmcnt(2)\n\t" \
    "s_add_u32 s62, s45, 9\n\t" \
    "s_and_b32 s62, s62, 7\n\t" \
    "s_add_u32 s62, s62, 16\n\t" \
    "s_lshl_b32 s63, s62, 8\n\t" \
    "s_add_u32 m0, s49, s63\n\t" \
    "s_lshl_b32 s62, s62, 16\n\t" \
    "s_add_u32 s52, s54, s62\n\t" \
    "s_addc_u32 s53, s55, 0\n\t" \
    "global_load_lds_dword v2, s[52:53]\n\t" \
    "s_waitcnt vmcnt(2)\n\t" \
    "ds_write_b32 v5, v7 offset:0\n\t" \
    "s_add_u32 s62, s45, 10\n\t" \
    "s_and_b32 s62, s62, 7\n\t" \
    "s_add_u32 s62, s62, 16\n\t" \
    "s_lshl_b32 s63, s62, 8\n\t" \
    "s_add_u32 m0, s49, s63\n\t" \
    "s_lshl_b32 s62, s62, 16\n\t" \
    "s_add_u32 s52, s54, s62\n\t" \
    "s_addc_u32 s53, s55, 0\n\t" \
    "global_load_lds_dword v2, s[52:53]\n\t" \
    "s_waitcnt vmcnt(2)\n\t" \
    "s_add_u32 s62, s45, 11\n\t" \
    "s_and_b32 s62, s62, 7\n\t" \
    "s_add_u32 s62, s62, 16\n\t" \
    "s_lshl_b32 s63, s62, 8\n\t" \
    "s_add_u32 m0, s49, s63\n\t" \
    "s_lshl_b32 s62, s62, 16\n\t" \
    "s_add_u32 s52, s54, s62\n\t" \
    "s_addc_u32 s53, s55, 0\n\t" \
    "global_load_lds_dword v2, s[52:53]\n\t" \
    "s_waitcnt vmcnt(2)\n\t" \
    "s_mov_b64 s[56:57], %14\n\t" \
    "s_add_u32 s56, s56, 0x630000\n\t" \
    "s_addc_u32 s57, s57, 0\n\t" \
    "s_lshl_b32 s42, s47, 12\n\t" \
    "s_add_u32 s56, s56, s42\n\t" \
    "s_addc_u32 s57, s57, 0\n\t" \
    "v_lshlrev_b32 v32, 4, v1\n\t" \
    "global_load_dwordx4 v[16:19], v32, s[56:57] sc1\n\t" \
    "global_load_dwordx4 v[20:23], v32, s[56:57] offset:1024 sc1\n\t" \
    "global_load_dwordx4 v[24:27], v32, s[56:57] offset:2048 sc1\n\t" \
    "global_load_dwordx4 v[28:31], v32, s[56:57] offset:3072 sc1\n\t" \
    "s_add_u32 s62, s45, 12\n\t" \
    "s_and_b32 s62, s62, 7\n\t" \
    "s_add_u32 s62, s62, 16\n\t" \
    "s_lshl_b32 s63, s62, 8\n\t" \
    "s_add_u32 m0, s49, s63\n\t" \
    "s_lshl_b32 s62, s62, 16\n\t" \
    "s_add_u32 s52, s54, s62\n\t" \
    "s_addc_u32 s53, s55, 0\n\t" \
    "global_load_lds_dword v2, s[52:53]\n\t" \
    "s_waitcnt vmcnt(6)\n\t" \
    "s_add_u32 s62, s45, 13\n\t" \
    "s_and_b32 s62, s62, 7\n\t" \
    "s_add_u32 s62, s62, 16\n\t" \
    "s_lshl_b32 s63, s62, 8\n\t" \
    "s_add_u32 m0, s49, s63\n\t" \
    "s_lshl_b32 s62, s62, 16\n\t" \
    "s_add_u32 s52, s54, s62\n\t" \
    "s_addc_u32 s53, s55, 0\n\t" \
    "global_load_lds_dword v2, s[52:53]\n\t" \
    "s_waitcnt vmcnt(6)\n\t" \
    "s_add_u32 s62, s45, 14\n\t" \
    "s_and_b32 s62, s62, 7\n\t" \
    "s_add_u32 s62, s62, 16\n\t" \
    "s_lshl_b32 s63, s62, 8\n\t" \
    "s_add_u32 m0, s49, s63\n\t" \
    "s_lshl_b32 s62, s62, 16\n\t" \
    "s_add_u32 s52, s54, s62\n\t" \
    "s_addc_u32 s53, s55, 0\n\t" \
    "global_load_lds_dword v2, s[52:53]\n\t" \
    "s_waitcnt vmcnt(2)\n\t" \
    "ds_write_b32 v5, v8 offset:0\n\t" \
    "s_add_u32 s62, s45, 15\n\t" \
    "s_and_b32 s62, s62, 7\n\t" \
    "s_add_u32 s62, s62, 16\n\t" \
    "s_lshl_b32 s63, s62, 8\n\t" \
    "s_add_u32 m0, s49, s63\n\t" \
    "s_lshl_b32 s62, s62, 16\n\t" \
    "s_add_u32 s52, s54, s62\n\t" \
    "s_addc_u32 s53, s55, 0\n\t" \
    "global_load_lds_dword v2, s[52:53]\n\t" \
    "v_mov_b32 v2, 0x1b254\n\t" \
    "s_mov_b32 s59, 0x100000\n\t" \
    ".Lsy6_%=:\n\t" \
    "ds_read_b32 v3, v2 offset:0\n\t" \
    "s_waitcnt lgkmcnt(0)\n\t" \
    "v_readfirstlane_b32 s42, v3\n\t" \
    "s_sub_u32 s59, s59, 1\n\t" \
    "s_cbranch_scc1 .Lsz6_%=\n\t" \
    "s_cmp_lt_u32 s42, 1\n\t" \
    "s_cbranch_scc1 .Lsy6_%=\n\t" \
    ".Lsz6_%=:\n\t" \
    "s_lshl_b32 s42, s47, 11\n\t" \
    "s_add_u32 s42, s42, 0x19240\n\t" \
    "v_lshl_add_u32 v33, v1, 3, s42\n\t" \
    "v_mov_b32 v2, 0x1b250\n\t" \
    "ds_read_b32 v34, v2 offset:0\n\t" \
    "s_lshl_b32 s42, s47, 10\n\t" \
    "s_add_u32 m0, s42, 0xe000\n\t" \
    "s_mov_b32 s59, 0x40000\n\t" \
    "s_waitcnt lgkmcnt(0)\n\t" \
    ".Lpl1_%=:\n\t" \
    "v_xor_b32 v4, v17, v34\n\t" \
    "v_bitop3_b32 v4, v19, v34, v4 bitop3:0xbe\n\t" \
    "v_bitop3_b32 v4, v21, v34, v4 bitop3:0xbe\n\t" \
    "v_bitop3_b32 v4, v23, v34, v4 bitop3:0xbe\n\t" \
    "v_bitop3_b32 v4, v25, v34, v4 bitop3:0xbe\n\t" \
    "v_bitop3_b32 v4, v27, v34, v4 bitop3:0xbe\n\t" \
    "v_bitop3_b32 v4, v29, v34, v4 bitop3:0xbe\n\t" \
    "v_bitop3_b32 v4, v31, v34, v4 bitop3:0xbe\n\t" \
    "v_cmp_ne_u32 vcc, 0, v4\n\t" \
    "s_and_b64 vcc, exec, vcc\n\t" \
    "s_cbranch_vccz .Lpk1_%=\n\t" \
    "s_sub_u32 s59, s59, 1\n\t" \
    "s_cbranch_scc1 .Lpk1_%=\n\t" \
    "global_load_dwordx4 v[16:19], v32, s[56:57] offset:0 sc1\n\t" \
    "global_load_dwordx4 v[20:23], v32, s[56:57] offset:1024 sc1\n\t" \
    "global_load_dwordx4 v[24:27], v32, s[56:57] offset:2048 sc1\n\t" \
    "global_load_dwordx4 v[28:31], v32, s[56:57] offset:3072 sc1\n\t" \
    "s_waitcnt vmcnt(0)\n\t" \
    "s_branch .Lpl1_%=\n\t" \
    ".Lpk1_%=:\n\t" \
    "v_and_b32 v17, 0xffff0000, v16\n\t" \
    "v_and_b32 v19, 0xffff0000, v18\n\t" \
    "v_and_b32 v21, 0xffff0000, v20\n\t" \
    "v_and_b32 v23, 0xffff0000, v22\n\t" \
    "v_and_b32 v25, 0xffff0000, v24\n\t" \
    "v_and_b32 v27, 0xffff0000, v26\n\t" \
    "v_and_b32 v29, 0xffff0000, v28\n\t" \
    "v_and_b32 v31, 0xffff0000, v30\n\t" \
    "v_rcp_f32 v17, v17\n\t" \
    "v_lshlrev_b32 v16, 16, v16\n\t" \
    "v_rcp_f32 v19, v19\n\t" \
    "v_lshlrev_b32 v18, 16, v18\n\t" \
    "v_rcp_f32 v21, v21\n\t" \
    "v_lshlrev_b32 v20, 16, v20\n\t" \
    "v_rcp_f32 v23, v23\n\t" \
    "v_lshlrev_b32 v22, 16, v22\n\t" \
    "v_rcp_f32 v25, v25\n\t" \
    "v_lshlrev_b32 v24, 16, v24\n\t" \
    "v_rcp_f32 v27, v27\n\t" \
    "v_lshlrev_b32 v26, 16, v26\n\t" \
    "v_rcp_f32 v29, v29\n\t" \
    "v_lshlrev_b32 v28, 16, v28\n\t" \
    "v_rcp_f32 v31, v31\n\t" \
    "v_lshlrev_b32 v30, 16, v30\n\t" \
    "v_mul_f32 v16, v16, v17\n\t" \
    "v_mul_f32 v18, v18, v19\n\t" \
    "v_mul_f32 v20, v20, v21\n\t" \
    "v_mul_f32 v22, v22, v23\n\t" \
    "v_mul_f32 v24, v24, v25\n\t" \
    "v_mul_f32 v26, v26, v27\n\t" \
    "v_mul_f32 v28, v28, v29\n\t" \
    "v_mul_f32 v30, v30, v31\n\t" \
    "s_waitcnt vmcnt(0)\n\t" \
    "s_setprio 3\n\t" \
    "ds_read_b64 v[36:37], v33 offset:0\n\t" \
    "ds_read_b64 v[40:41], v33 offset:512\n\t" \
    "ds_read_b64 v[44:45], v33 offset:1024\n\t" \
    "ds_read_b64 v[48:49], v33 offset:1536\n\t" \
    "s_waitcnt lgkmcnt(3)\n\t" \
    "v_and_b32 v39, 0xffff0000, v37\n\t" \
    "v_lshlrev_b32 v38, 16, v37\n\t" \
    "v_and_b32 v37, 0xffff0000, v36\n\t" \
    "v_lshlrev_b32 v36, 16, v36\n\t" \
    "v_fma_f32 v36, v36, v17, -v16\n\t" \
    "v_fma_f32 v37, v37, v17, -v16\n\t" \
    "v_fma_f32 v38, v38, v19, -v18\n\t" \
    "v_fma_f32 v39, v39, v19, -v18\n\t" \
    "v_cvt_pk_bf16_f32 v36, v36, v38\n\t" \
    "ds_write_addtid_b32 v36 offset:25696\n\t" \
    "v_cvt_pk_bf16_f32 v37, v37, v39\n\t" \
    "ds_write_addtid_b32 v37 offset:30272\n\t" \
    "s_waitcnt lgkmcnt(4)\n\t" \
    "v_and_b32 v43, 0xffff0000, v41\n\t" \
    "v_lshlrev_b32 v42, 16, v41\n\t" \
    "v_and_b32 v41, 0xffff0000, v40\n\t" \
    "v_lshlrev_b32 v40, 16, v40\n\t" \
    "v_fma_f32 v40, v40, v21, -v20\n\t" \
    "v_fma_f32 v41, v41, v21, -v20\n\t" \
    "v_fma_f32 v42, v42, v23, -v22\n\t" \
    "v_fma_f32 v43, v43, v23, -v22\n\t" \
    "v_cvt_pk_bf16_f32 v40, v40, v42\n\t" \
    "ds_write_addtid_b32 v40 offset:25952\n\t" \
    "v_cvt_pk_bf16_f32 v41, v41, v43\n\t" \
    "ds_write_addtid_b32 v41 offset:30528\n\t" \
    "s_waitcnt lgkmcnt(5)\n\t" \
    "v_and_b32 v47, 0xffff0000, v45\n\t" \
    "v_lshlrev_b32 v46, 16, v45\n\t" \
    "v_and_b32 v45, 0xffff0000, v44\n\t" \
    "v_lshlrev_b32 v44, 16, v44\n\t" \
    "v_fma_f32 v44, v44, v25, -v24\n\t" \
    "v_fma_f32 v45, v45, v25, -v24\n\t" \
    "v_fma_f32 v46, v46, v27, -v26\n\t" \
    "v_fma_f32 v47, v47, v27, -v26\n\t" \
    "v_cvt_pk_bf16_f32 v44, v44, v46\n\t" \
    "ds_write_addtid_b32 v44 offset:26208\n\t" \
    "v_cvt_pk_bf16_f32 v45, v45, v47\n\t" \
    "ds_write_addtid_b32 v45 offset:30784\n\t" \
    "s_waitcnt lgkmcnt(6)\n\t" \
    "v_and_b32 v51, 0xffff0000, v49\n\t" \
    "v_lshlrev_b32 v50, 16, v49\n\t" \
    "v_and_b32 v49, 0xffff0000, v48\n\t" \
    "v_lshlrev_b32 v48, 16, v48\n\t" \
    "v_fma_f32 v48, v48, v29, -v28\n\t" \
    "v_fma_f32 v49, v49, v29, -v28\n\t" \
    "v_fma_f32 v50, v50, v31, -v30\n\t" \
    "v_fma_f32 v51, v51, v31, -v30\n\t" \
    "v_cvt_pk_bf16_f32 v48, v48, v50\n\t" \
    "ds_write_addtid_b32 v48 offset:26464\n\t" \
    "v_cvt_pk_bf16_f32 v49, v49, v51\n\t" \
    "ds_write_addtid_b32 v49 offset:31040\n\t" \
    "s_waitcnt lgkmcnt(0)\n\t" \
    "s_setprio 0\n\t" \
    "s_branch .Lh1b_%=\n\t" \
    ".Lcomp_%=:\n\t" \
    "v_mov_b32 v10, 0\n\t" \
    "v_mov_b32 v11, 0\n\t" \
    "v_mov_b32 v12, 0\n\t" \
    "v_mov_b32 v13, 0\n\t" \
    "v_mov_b32 v14, 0\n\t" \
    "v_mov_b32 v15, 0\n\t" \
    "s_lshl_b32 s40, s44, 9\n\t" \
    "s_lshl_b32 s41, %1, 7\n\t" \
    "s_add_u32 s40, s40, s41\n\t" \
    "v_lshl_add_u32 v2, v1, 1, s40\n\t" \
    "global_load_short_d16_hi v10, v2, %3\n\t" \
    "global_load_short_d16_hi v11, v2, %4\n\t" \
    "global_load_short_d16_hi v12, v2, %5\n\t" \
    "global_load_short_d16_hi v13, v2, %6\n\t" \
    "global_load_short_d16_hi v14, v2, %7\n\t" \
    "global_load_short_d16_hi v15, v2, %8\n\t" \
    "s_lshl_b32 s42, s44, 2\n\t" \
    "s_load_dword s48, %9, s42\n\t" \
    "s_load_dword s49, %10, s42\n\t" \
    "s_lshr_b32 s50, %1, 1\n\t" \
    "s_and_b32 s51, %1, 1\n\t" \
    "s_lshl_b32 s42, s51, 10\n\t" \
    "v_lshl_add_u32 v3, v1, 4, s42\n\t" \
    "global_load_dwordx4 v[52:55], v3, %17\n\t" \
    "global_load_dwordx2 v[56:57], v3, %17 offset:16\n\t" \
    "s_barrier\n\t" \
    "s_mul_i32 s52, s50, 5632\n\t" \
    "s_add_u32 s52, s52, 0x16640\n\t" \
    "s_mul_i32 s40, %1, 16416\n\t" \
    "s_mov_b32 m0, s40\n\t" \
    "v_and_b32 v5, 15, v1\n\t" \
    "v_lshrrev_b32 v6, 4, v1\n\t" \
    "v_mul_u32_u24 v7, 0x120, v5\n\t" \
    "v_lshl_add_u32 v7, v6, 4, v7\n\t" \
    "v_add_u32 v58, s40, v7\n\t" \
    "s_mul_i32 s41, s51, 2816\n\t" \
    "s_add_u32 s41, s41, s52\n\t" \
    "v_lshlrev_b32 v7, 2, v6\n\t" \
    "v_lshl_add_u32 v8, v5, 4, v7\n\t" \
    "v_lshl_add_u32 v59, v8, 2, s41\n\t" \
    "v_lshl_add_u32 v8, v5, 3, v7\n\t" \
    "v_add_u32 v8, 0x100, v8\n\t" \
    "v_lshlrev_b32 v9, 6, v6\n\t" \
    "v_lshl_add_u32 v16, v5, 2, v9\n\t" \
    "v_add_u32 v16, 0x100, v16\n\t" \
    "v_cmp_gt_u32 vcc, 32, v1\n\t" \
    "v_lshl_add_u32 v9, v5, 1, v9\n\t" \
    "v_add_u32 v9, 0x200, v9\n\t" \
    "v_cndmask_b32 v8, v16, v8, vcc\n\t" \
    "v_lshl_add_u32 v60, v8, 2, s41\n\t" \
    "v_mov_b32 v8, 0x264\n\t" \
    "v_cndmask_b32 v9, v8, v9, vcc\n\t" \
    "v_lshl_add_u32 v61, v9, 2, s41\n\t" \
    "s_mul_i32 s40, s50, 8448\n\t" \
    "s_add_u32 s40, s40, 0x10080\n\t" \
    "s_lshl_b32 s42, s51, 10\n\t" \
    "s_add_u32 s42, s42, s40\n\t" \
    "v_lshl_add_u32 v62, v1, 4, s42\n\t" \
    "v_sub_u32 v7, 0, v5\n\t" \
    "v_and_b32 v7, 3, v7\n\t" \
    "v_mul_u32_u24 v8, 0x83e, v7\n\t" \
    "v_lshlrev_b32 v16, 1, v5\n\t" \
    "v_sub_u32 v8, v8, v16\n\t" \
    "v_lshlrev_b32 v9, 4, v6\n\t" \
    "s_lshl_b32 s42, s51, 8\n\t" \
    "s_sub_u32 s43, s40, s42\n\t" \
    "s_add_u32 s43, s43, 352\n\t" \
    "v_add_u32 v16, v8, v9\n\t" \
    "v_add_u32 v92, s43, v16\n\t" \
    "s_add_u32 s43, s40, s42\n\t" \
    "v_lshl_add_u32 v16, v6, 3, v8\n\t" \
    "v_add_u32 v67, s43, v16\n\t" \
    "s_mul_i32 s43, s50, 4576\n\t" \
    "s_add_u32 s53, s43, 0x14280\n\t" \
    "v_lshl_add_u32 v8, v5, 5, v9\n\t" \
    "v_add_u32 v93, s53, v8\n\t" \
    "s_mul_i32 s43, %1, 10240\n\t" \
    "v_mul_u32_u24 v8, 0x140, v6\n\t" \
    "v_lshl_add_u32 v8, v5, 4, v8\n\t" \
    "v_add_u32 v94, s43, v8\n\t" \
    "s_lshl_b32 s42, %1, 2\n\t" \
    "s_add_u32 s54, s42, 0x1b240\n\t" \
    "s_xor_b32 s55, s54, 4\n\t" \
    "v_mov_b32 v63, s54\n\t" \
    "v_mov_b32 v51, s55\n\t" \
    "v_mov_b32 v3, s52\n\t" \
    "s_waitcnt lgkmcnt(0)\n\t" \
    "s_lshl_b32 s42, s48, 16\n\t" \
    "s_and_b32 s43, s48, 0xffff0000\n\t" \
    "s_cmp_eq_u32 s50, 0\n\t" \
    "s_cselect_b32 s42, s42, s43\n\t" \
    "v_mov_b32 v64, 0x3fb8aa3b\n\t" \
    "v_mul_f32 v64, s42, v64\n\t" \
    "v_exp_f32 v64, v64\n\t" \
    "s_nop 0\n\t" \
    "v_cvt_pk_bf16_f32 v64, v64, v64\n\t" \
    "v_and_b32 v64, 0xffff0000, v64\n\t" \
    "s_waitcnt vmcnt(2)\n\t" \
    "v_mul_f32 v10, v64, v10\n\t" \
    "v_mul_f32 v11, v64, v11\n\t" \
    "v_mul_f32 v12, v64, v12\n\t" \
    "v_mul_f32 v13, v64, v13\n\t" \
    "v_mul_f32 v65, 0x3fb8aa3b, v10\n\t" \
    "v_mul_f32 v66, 0x3e22f983, v11\n\t" \
    "v_xor_b32 v71, 0x80000000, v13\n\t" \
    "v_mul_f32 v19, 0x41800000, v65\n\t" \
    "v_mul_f32 v20, 0x41800000, v66\n\t" \
    "v_mul_f32 v22, 0x43800000, v65\n\t" \
    "v_mul_f32 v23, 0x43800000, v66\n\t" \
    "v_mul_f32 v68, v15, v71\n\t" \
    "v_mul_f32 v69, v15, v12\n\t" \
    "v_exp_f32 v18, v65\n\t" \
    "v_fract_f32 v17, v66\n\t" \
    "v_exp_f32 v21, v19\n\t" \
    "v_fract_f32 v20, v20\n\t" \
    "v_exp_f32 v24, v22\n\t" \
    "v_fract_f32 v23, v23\n\t" \
    "v_fmac_f32 v68, v14, v12\n\t" \
    "v_fmac_f32 v69, v14, v13\n\t" \
    "v_cos_f32 v16, v17\n\t" \
    "v_sin_f32 v17, v17\n\t" \
    "v_cos_f32 v19, v20\n\t" \
    "v_sin_f32 v20, v20\n\t" \
    "v_cos_f32 v22, v23\n\t" \
    "v_sin_f32 v23, v23\n\t" \
    "v_and_b32 v70, 0x7fffffff, v68\n\t" \
    "v_and_b32 v71, 0x7fffffff, v69\n\t" \
    "v_mul_f32 v16, v16, v18\n\t" \
    "v_mul_f32 v17, v17, v18\n\t" \
    "v_mul_f32 v19, v19, v21\n\t" \
    "v_mul_f32 v20, v20, v21\n\t" \
    "v_mul_f32 v22, v22, v24\n\t" \
    "v_mul_f32 v23, v23, v24\n\t" \
    "v_max_u32 v70, v70, v71\n\t" \
    "v_xor_b32 v18, 0x80000000, v17\n\t" \
    "v_xor_b32 v21, 0x80000000, v20\n\t" \
    "v_xor_b32 v24, 0x80000000, v23\n\t" \
    "v_max_u32_dpp v70, v70, v70 row_shr:1 row_mask:0xf bank_mask:0xf bound_ctrl:1\n\t" \
    "v_mul_f32 v25, v17, v18\n\t" \
    "v_add_f32 v26, v16, v16\n\t" \
    "v_mul_f32 v28, v20, v21\n\t" \
    "v_add_f32 v29, v19, v19\n\t" \
    "v_max_u32_dpp v70, v70, v70 row_shr:2 row_mask:0xf bank_mask:0xf bound_ctrl:1\n\t" \
    "v_fmac_f32 v25, v16, v16\n\t" \
    "v_mul_f32 v26, v26, v17\n\t" \
    "v_fmac_f32 v28, v19, v19\n\t" \
    "v_mul_f32 v29, v29, v20\n\t" \
    "v_max_u32_dpp v70, v70, v70 row_shr:4 row_mask:0xf bank_mask:0xf bound_ctrl:1\n\t" \
    "v_xor_b32 v27, 0x80000000, v26\n\t" \
    "v_xor_b32 v30, 0x80000000, v29\n\t" \
    "v_max_u32_dpp v70, v70, v70 row_shr:8 row_mask:0xf bank_mask:0xf bound_ctrl:1\n\t" \
    "v_mul_f32 v72, v26, v27\n\t" \
    "v_add_f32 v73, v25, v25\n\t" \
    "v_mul_f32 v81, v23, v24\n\t" \
    "v_add_f32 v82, v22, v22\n\t" \
    "v_max_u32_dpp v70, v70, v70 row_bcast:15 row_mask:0xa bank_mask:0xf\n\t" \
    "v_fmac_f32 v72, v25, v25\n\t" \
    "v_mul_f32 v73, v73, v26\n\t" \
    "v_fmac_f32 v81, v22, v22\n\t" \
    "v_mul_f32 v82, v82, v23\n\t" \
    "v_max_u32_dpp v70, v70, v70 row_bcast:31 row_mask:0xc bank_mask:0xf\n\t" \
    "v_xor_b32 v74, 0x80000000, v73\n\t" \
    "v_xor_b32 v83, 0x80000000, v82\n\t" \
    "v_readlane_b32 s42, v70, 63\n\t" \
    "v_mul_f32 v75, v73, v74\n\t" \
    "v_add_f32 v76, v72, v72\n\t" \
    "v_fmac_f32 v75, v72, v72\n\t" \
    "v_mul_f32 v76, v76, v73\n\t" \
    "v_xor_b32 v77, 0x80000000, v76\n\t" \
    "s_lshr_b32 s42, s42, 23\n\t" \
    "s_max_u32 s42, s42, 1\n\t" \
    "s_min_u32 s42, s42, 253\n\t" \
    "s_lshl_b32 s58, s42, 23\n\t" \
    "s_sub_u32 s42, 0x7f000000, s58\n\t" \
    "v_mul_f32 v78, v76, v74\n\t" \
    "v_mul_f32 v79, v76, v72\n\t" \
    "v_fmac_f32 v78, v75, v72\n\t" \
    "v_fmac_f32 v79, v75, v73\n\t" \
    "v_xor_b32 v80, 0x80000000, v79\n\t" \
    "v_mul_f32 v68, s42, v68\n\t" \
    "v_mul_f32 v69, s42, v69\n\t" \
    "v_mul_f32 v96, v69, v18\n\t" \
    "v_mul_f32 v97, v69, v16\n\t" \
    "v_fmac_f32 v96, v68, v16\n\t" \
    "v_fmac_f32 v97, v68, v17\n\t" \
    "v_mov_b32 v9, 0x3c00\n\t" \
    "ds_write_addtid_b32 v9 offset:11808\n\t" \
    "v_mul_f32 v98, v69, v83\n\t" \
    "v_mul_f32 v99, v69, v81\n\t" \
    "v_fmac_f32 v98, v68, v81\n\t" \
    "v_fmac_f32 v99, v68, v82\n\t" \
    "v_cvt_pk_f16_f32 v208, v68, v69\n\t" \
    "v_cvt_pk_f16_f32 v209, v96, v97\n\t" \
    "v_mul_f32 v100, v69, v24\n\t" \
    "v_mul_f32 v101, v69, v22\n\t" \
    "v_cvt_pk_f16_f32 v211, v19, -v20\n\t" \
    "v_mul_f32 v102, v69, v27\n\t" \
    "v_mul_f32 v103, v69, v25\n\t" \
    "v_mul_f32 v104, v97, v27\n\t" \
    "v_mul_f32 v105, v97, v25\n\t" \
    "v_mul_f32 v106, v20, v30\n\t" \
    "v_mul_f32 v107, v20, v28\n\t" \
    "v_fmac_f32 v100, v68, v22\n\t" \
    "ds_write_addtid_b32 v208 offset:0\n\t" \
    "v_fmac_f32 v101, v68, v23\n\t" \
    "v_fmac_f32 v102, v68, v25\n\t" \
    "v_fmac_f32 v103, v68, v26\n\t" \
    "v_fmac_f32 v104, v96, v25\n\t" \
    "v_fmac_f32 v105, v96, v26\n\t" \
    "ds_write_addtid_b32 v209 offset:288\n\t" \
    "v_fmac_f32 v106, v19, v28\n\t" \
    "v_fmac_f32 v107, v19, v29\n\t" \
    "v_cvt_pk_f16_f32 v210, v100, v101\n\t" \
    "v_cvt_pk_f16_f32 v212, v102, v103\n\t" \
    "v_cvt_pk_f16_f32 v213, v104, v105\n\t" \
    "ds_write_addtid_b32 v211 offset:12096\n\t" \
    "v_mul_f32 v108, v103, v24\n\t" \
    "v_mul_f32 v109, v103, v22\n\t" \
    "v_cvt_pk_f16_f32 v215, v106, -v107\n\t" \
    "v_cvt_pk_f16_f32 v216, v28, -v29\n\t" \
    "v_mul_f32 v110, v103, v27\n\t" \
    "ds_write_addtid_b32 v210 offset:4608\n\t" \
    "v_mul_f32 v111, v103, v25\n\t" \
    "v_mul_f32 v112, v105, v27\n\t" \
    "v_mul_f32 v113, v105, v25\n\t" \
    "v_mul_f32 v114, v107, v30\n\t" \
    "v_mul_f32 v115, v107, v28\n\t" \
    "v_mul_f32 v116, v29, v30\n\t" \
    "v_mul_f32 v117, v29, v28\n\t" \
    "v_fmac_f32 v108, v102, v22\n\t" \
    "ds_write_addtid_b32 v212 offset:576\n\t" \
    "v_fmac_f32 v109, v102, v23\n\t" \
    "v_fmac_f32 v110, v102, v25\n\t" \
    "v_fmac_f32 v111, v102, v26\n\t" \
    "v_fmac_f32 v112, v104, v25\n\t" \
    "v_fmac_f32 v113, v104, v26\n\t" \
    "ds_write_addtid_b32 v213 offset:864\n\t" \
    "v_fmac_f32 v114, v106, v28\n\t" \
    "v_fmac_f32 v115, v106, v29\n\t" \
    "v_fmac_f32 v116, v28, v28\n\t" \
    "v_fmac_f32 v117, v28, v29\n\t" \
    "v_cvt_pk_f16_f32 v214, v108, v109\n\t" \
    "ds_write_addtid_b32 v215 offset:12672\n\t" \
    "v_cvt_pk_f16_f32 v217, v110, v111\n\t" \
    "v_cvt_pk_f16_f32 v218, v112, v113\n\t" \
    "v_mul_f32 v118, v111, v24\n\t" \
    "v_mul_f32 v119, v111, v22\n\t" \
    "v_cvt_pk_f16_f32 v220, v114, -v115\n\t" \
    "ds_write_addtid_b32 v216 offset:12384\n\t" \
    "v_cvt_pk_f16_f32 v221, v116, -v117\n\t" \
    "v_mul_f32 v120, v111, v27\n\t" \
    "v_mul_f32 v121, v111, v25\n\t" \
    "v_mul_f32 v122, v113, v27\n\t" \
    "v_mul_f32 v123, v113, v25\n\t" \
    "ds_write_addtid_b32 v214 offset:4896\n\t" \
    "v_mul_f32 v124, v115, v30\n\t" \
    "v_mul_f32 v125, v115, v28\n\t" \
    "v_mul_f32 v126, v117, v30\n\t" \
    "v_mul_f32 v127, v117, v28\n\t" \
    "v_fmac_f32 v118, v110, v22\n\t" \
    "ds_write_addtid_b32 v217 offset:1152\n\t" \
    "v_fmac_f32 v119, v110, v23\n\t" \
    "v_fmac_f32 v120, v110, v25\n\t" \
    "v_fmac_f32 v121, v110, v26\n\t" \
    "v_fmac_f32 v122, v112, v25\n\t" \
    "v_fmac_f32 v123, v112, v26\n\t" \
    "ds_write_addtid_b32 v218 offset:1440\n\t" \
    "v_fmac_f32 v124, v114, v28\n\t" \
    "v_fmac_f32 v125, v114, v29\n\t" \
    "v_fmac_f32 v126, v116, v28\n\t" \
    "v_fmac_f32 v127, v116, v29\n\t" \
    "v_cvt_pk_f16_f32 v219, v118, v119\n\t" \
    "ds_write_addtid_b32 v220 offset:13248\n\t" \
    "v_cvt_pk_f16_f32 v222, v120, v121\n\t" \
    "v_cvt_pk_f16_f32 v223, v122, v123\n\t" \
    "v_mul_f32 v128, v121, v24\n\t" \
    "v_mul_f32 v129, v121, v22\n\t" \
    "v_cvt_pk_f16_f32 v225, v124, -v125\n\t" \
    "ds_write_addtid_b32 v221 offset:12960\n\t" \
    "v_cvt_pk_f16_f32 v226, v126, -v127\n\t" \
    "v_mul_f32 v130, v121, v27\n\t" \
    "v_mul_f32 v131, v121, v25\n\t" \
    "v_mul_f32 v132, v123, v27\n\t" \
    "v_mul_f32 v133, v123, v25\n\t" \
    "ds_write_addtid_b32 v219 offset:5184\n\t" \
    "v_mul_f32 v134, v125, v30\n\t" \
    "v_mul_f32 v135, v125, v28\n\t" \
    "v_mul_f32 v136, v127, v30\n\t" \
    "v_mul_f32 v137, v127, v28\n\t" \
    "v_fmac_f32 v128, v120, v22\n\t" \
    "ds_write_addtid_b32 v222 offset:1728\n\t" \
    "v_fmac_f32 v129, v120, v23\n\t" \
    "v_fmac_f32 v130, v120, v25\n\t" \
    "v_fmac_f32 v131, v120, v26\n\t" \
    "v_fmac_f32 v132, v122, v25\n\t" \
    "v_fmac_f32 v133, v122, v26\n\t" \
    "ds_write_addtid_b32 v223 offset:2016\n\t" \
    "v_fmac_f32 v134, v124, v28\n\t" \
    "v_fmac_f32 v135, v124, v29\n\t" \
    "v_fmac_f32 v136, v126, v28\n\t" \
    "v_fmac_f32 v137, v126, v29\n\t" \
    "v_cvt_pk_f16_f32 v224, v128, v129\n\t" \
    "ds_write_addtid_b32 v225 offset:13824\n\t" \
    "v_cvt_pk_f16_f32 v227, v130, v131\n\t" \
    "v_cvt_pk_f16_f32 v228, v132, v133\n\t" \
    "v_mul_f32 v138, v131, v24\n\t" \
    "v_mul_f32 v139, v131, v22\n\t" \
    "v_cvt_pk_f16_f32 v230, v134, -v135\n\t" \
    "ds_write_addtid_b32 v226 offset:13536\n\t" \
    "v_cvt_pk_f16_f32 v231, v136, -v137\n\t" \
    "v_mul_f32 v140, v131, v27\n\t" \
    "v_mul_f32 v141, v131, v25\n\t" \
    "v_mul_f32 v142, v133, v27\n\t" \
    "v_mul_f32 v143, v133, v25\n\t" \
    "ds_write_addtid_b32 v224 offset:5472\n\t" \
    "v_mul_f32 v144, v135, v30\n\t" \
    "v_mul_f32 v145, v135, v28\n\t" \
    "v_mul_f32 v146, v137, v30\n\t" \
    "v_mul_f32 v147, v137, v28\n\t" \
    "v_fmac_f32 v138, v130, v22\n\t" \
    "ds_write_addtid_b32 v227 offset:2304\n\t" \
    "v_fmac_f32 v139, v130, v23\n\t" \
    "v_fmac_f32 v140, v130, v25\n\t" \
    "v_fmac_f32 v141, v130, v26\n\t" \
    "v_fmac_f32 v142, v132, v25\n\t" \
    "v_fmac_f32 v143, v132, v26\n\t" \
    "ds_write_addtid_b32 v228 offset:2592\n\t" \
    "v_fmac_f32 v144, v134, v28\n\t" \
    "v_fmac_f32 v145, v134, v29\n\t" \
    "v_fmac_f32 v146, v136, v28\n\t" \
    "v_fmac_f32 v147, v136, v29\n\t" \
    "v_cvt_pk_f16_f32 v229, v138, v139\n\t" \
    "ds_write_addtid_b32 v230 offset:14400\n\t" \
    "v_cvt_pk_f16_f32 v232, v140, v141\n\t" \
    "v_cvt_pk_f16_f32 v233, v142, v143\n\t" \
    "v_mul_f32 v148, v141, v24\n\t" \
    "v_mul_f32 v149, v141, v22\n\t" \
    "v_cvt_pk_f16_f32 v235, v144, -v145\n\t" \
    "ds_write_addtid_b32 v231 offset:14112\n\t" \
    "v_cvt_pk_f16_f32 v236, v146, -v147\n\t" \
    "v_mul_f32 v150, v141, v27\n\t" \
    "v_mul_f32 v151, v141, v25\n\t" \
    "v_mul_f32 v152, v143, v27\n\t" \
    "v_mul_f32 v153, v143, v25\n\t" \
    "ds_write_addtid_b32 v229 offset:5760\n\t" \
    "v_mul_f32 v154, v145, v30\n\t" \
    "v_mul_f32 v155, v145, v28\n\t" \
    "v_mul_f32 v156, v147, v30\n\t" \
    "v_mul_f32 v157, v147, v28\n\t" \
    "v_fmac_f32 v148, v140, v22\n\t" \
    "ds_write_addtid_b32 v232 offset:2880\n\t" \
    "v_fmac_f32 v149, v140, v23\n\t" \
    "v_fmac_f32 v150, v140, v25\n\t" \
    "v_fmac_f32 v151, v140, v26\n\t" \
    "v_fmac_f32 v152, v142, v25\n\t" \
    "v_fmac_f32 v153, v142, v26\n\t" \
    "ds_write_addtid_b32 v233 offset:3168\n\t" \
    "v_fmac_f32 v154, v144, v28\n\t" \
    "v_fmac_f32 v155, v144, v29\n\t" \
    "v_fmac_f32 v156, v146, v28\n\t" \
    "v_fmac_f32 v157, v146, v29\n\t" \
    "v_cvt_pk_f16_f32 v234, v148, v149\n\t" \
    "ds_write_addtid_b32 v235 offset:14976\n\t" \
    "v_cvt_pk_f16_f32 v237, v150, v151\n\t" \
    "v_cvt_pk_f16_f32 v238, v152, v153\n\t" \
    "v_mul_f32 v158, v151, v24\n\t" \
    "v_mul_f32 v159, v151, v22\n\t" \
    "v_cvt_pk_f16_f32 v240, v154, -v155\n\t" \
    "ds_write_addtid_b32 v236 offset:14688\n\t" \
    "v_cvt_pk_f16_f32 v241, v156, -v157\n\t" \
    "v_mul_f32 v160, v151, v27\n\t" \
    "v_mul_f32 v161, v151, v25\n\t" \
    "v_mul_f32 v162, v153, v27\n\t" \
    "v_mul_f32 v163, v153, v25\n\t" \
    "ds_write_addtid_b32 v234 offset:6048\n\t" \
    "v_mul_f32 v164, v155, v30\n\t" \
    "v_mul_f32 v165, v155, v28\n\t" \
    "v_mul_f32 v166, v157, v30\n\t" \
    "v_mul_f32 v167, v157, v28\n\t" \
    "v_fmac_f32 v158, v150, v22\n\t" \
    "ds_write_addtid_b32 v237 offset:3456\n\t" \
    "v_fmac_f32 v159, v150, v23\n\t" \
    "v_fmac_f32 v160, v150, v25\n\t" \
    "v_fmac_f32 v161, v150, v26\n\t" \
    "v_fmac_f32 v162, v152, v25\n\t" \
    "v_fmac_f32 v163, v152, v26\n\t" \
    "ds_write_addtid_b32 v238 offset:3744\n\t" \
    "v_fmac_f32 v164, v154, v28\n\t" \
    "v_fmac_f32 v165, v154, v29\n\t" \
    "v_fmac_f32 v166, v156, v28\n\t" \
    "v_fmac_f32 v167, v156, v29\n\t" \
    "v_cvt_pk_f16_f32 v239, v158, v159\n\t" \
    "ds_write_addtid_b32 v240 offset:15552\n\t" \
    "v_cvt_pk_f16_f32 v242, v160, v161\n\t" \
    "v_cvt_pk_f16_f32 v243, v162, v163\n\t" \
    "v_mul_f32 v168, v161, v24\n\t" \
    "v_mul_f32 v169, v161, v22\n\t" \
    "v_cvt_pk_f16_f32 v245, v164, -v165\n\t" \
    "ds_write_addtid_b32 v241 offset:15264\n\t" \
    "v_cvt_pk_f16_f32 v246, v166, -v167\n\t" \
    "v_fmac_f32 v168, v160, v22\n\t" \
    "v_fmac_f32 v169, v160, v23\n\t" \
    "v_cvt_pk_f16_f32 v244, v168, v169\n\t" \
    "v_cvt_pk_f16_f32 v247, v98, v99\n\t" \
    "ds_write_addtid_b32 v239 offset:6336\n\t" \
    "v_mul_f32 v170, v99, v74\n\t" \
    "v_mul_f32 v171, v99, v72\n\t" \
    "v_mul_f32 v172, v99, v77\n\t" \
    "v_mul_f32 v173, v99, v75\n\t" \
    "v_mul_f32 v174, v99, v80\n\t" \
    "ds_write_addtid_b32 v242 offset:4032\n\t" \
    "v_mul_f32 v175, v99, v78\n\t" \
    "v_mul_f32 v176, v99, v24\n\t" \
    "v_mul_f32 v177, v99, v22\n\t" \
    "v_fmac_f32 v170, v98, v72\n\t" \
    "v_fmac_f32 v171, v98, v73\n\t" \
    "ds_write_addtid_b32 v243 offset:4320\n\t" \
    "v_fmac_f32 v172, v98, v75\n\t" \
    "v_fmac_f32 v173, v98, v76\n\t" \
    "v_fmac_f32 v174, v98, v78\n\t" \
    "v_fmac_f32 v175, v98, v79\n\t" \
    "v_fmac_f32 v176, v98, v22\n\t" \
    "ds_write_addtid_b32 v245 offset:16128\n\t" \
    "v_fmac_f32 v177, v98, v23\n\t" \
    "v_cvt_pk_f16_f32 v208, v170, v171\n\t" \
    "v_cvt_pk_f16_f32 v209, v172, v173\n\t" \
    "v_cvt_pk_f16_f32 v210, v174, v175\n\t" \
    "v_cvt_pk_f16_f32 v211, v176, v177\n\t" \
    "ds_write_addtid_b32 v246 offset:15840\n\t" \
    "v_mul_f32 v178, v177, v74\n\t" \
    "v_mul_f32 v179, v177, v72\n\t" \
    "v_mul_f32 v180, v177, v77\n\t" \
    "v_mul_f32 v181, v177, v75\n\t" \
    "v_mul_f32 v182, v177, v80\n\t" \
    "ds_write_addtid_b32 v244 offset:6624\n\t" \
    "v_mul_f32 v183, v177, v78\n\t" \
    "v_mul_f32 v184, v177, v24\n\t" \
    "v_mul_f32 v185, v177, v22\n\t" \
    "v_fmac_f32 v178, v176, v72\n\t" \
    "v_fmac_f32 v179, v176, v73\n\t" \
    "ds_write_addtid_b32 v247 offset:6912\n\t" \
    "v_fmac_f32 v180, v176, v75\n\t" \
    "v_fmac_f32 v181, v176, v76\n\t" \
    "v_fmac_f32 v182, v176, v78\n\t" \
    "v_fmac_f32 v183, v176, v79\n\t" \
    "v_fmac_f32 v184, v176, v22\n\t" \
    "ds_write_addtid_b32 v208 offset:7200\n\t" \
    "v_fmac_f32 v185, v176, v23\n\t" \
    "v_cvt_pk_f16_f32 v212, v178, v179\n\t" \
    "v_cvt_pk_f16_f32 v213, v180, v181\n\t" \
    "v_cvt_pk_f16_f32 v214, v182, v183\n\t" \
    "v_cvt_pk_f16_f32 v215, v184, v185\n\t" \
    "ds_write_addtid_b32 v209 offset:7488\n\t" \
    "v_mul_f32 v186, v185, v77\n\t" \
    "v_mul_f32 v187, v185, v75\n\t" \
    "v_mul_f32 v188, v185, v24\n\t" \
    "v_mul_f32 v189, v185, v22\n\t" \
    "v_fmac_f32 v186, v184, v75\n\t" \
    "ds_write_addtid_b32 v210 offset:7776\n\t" \
    "v_fmac_f32 v187, v184, v76\n\t" \
    "v_fmac_f32 v188, v184, v22\n\t" \
    "v_fmac_f32 v189, v184, v23\n\t" \
    "v_cvt_pk_f16_f32 v216, v186, v187\n\t" \
    "v_cvt_pk_f16_f32 v217, v188, v189\n\t" \
    "ds_write_addtid_b32 v211 offset:8064\n\t" \
    "v_mul_f32 v190, v189, v77\n\t" \
    "v_mul_f32 v191, v189, v75\n\t" \
    "v_mul_f32 v192, v189, v24\n\t" \
    "v_mul_f32 v193, v189, v22\n\t" \
    "v_fmac_f32 v190, v188, v75\n\t" \
    "ds_write_addtid_b32 v212 offset:8352\n\t" \
    "v_fmac_f32 v191, v188, v76\n\t" \
    "v_fmac_f32 v192, v188, v22\n\t" \
    "v_fmac_f32 v193, v188, v23\n\t" \
    "v_cvt_pk_f16_f32 v218, v190, v191\n\t" \
    "v_cvt_pk_f16_f32 v219, v192, v193\n\t" \
    "ds_write_addtid_b32 v213 offset:8640\n\t" \
    "v_mul_f32 v194, v193, v77\n\t" \
    "v_mul_f32 v195, v193, v75\n\t" \
    "v_mul_f32 v196, v193, v24\n\t" \
    "v_mul_f32 v197, v193, v22\n\t" \
    "v_fmac_f32 v194, v192, v75\n\t" \
    "ds_write_addtid_b32 v214 offset:8928\n\t" \
    "v_fmac_f32 v195, v192, v76\n\t" \
    "v_fmac_f32 v196, v192, v22\n\t" \
    "v_fmac_f32 v197, v192, v23\n\t" \
    "v_cvt_pk_f16_f32 v220, v194, v195\n\t" \
    "v_cvt_pk_f16_f32 v221, v196, v197\n\t" \
    "ds_write_addtid_b32 v215 offset:9216\n\t" \
    "v_mul_f32 v198, v197, v77\n\t" \
    "v_mul_f32 v199, v197, v75\n\t" \
    "v_mul_f32 v200, v197, v24\n\t" \
    "v_mul_f32 v201, v197, v22\n\t" \
    "v_fmac_f32 v198, v196, v75\n\t" \
    "ds_write_addtid_b32 v216 offset:9504\n\t" \
    "v_fmac_f32 v199, v196, v76\n\t" \
    "v_fmac_f32 v200, v196, v22\n\t" \
    "v_fmac_f32 v201, v196, v23\n\t" \
    "v_cvt_pk_f16_f32 v222, v198, v199\n\t" \
    "v_cvt_pk_f16_f32 v223, v200, v201\n\t" \
    "ds_write_addtid_b32 v217 offset:9792\n\t" \
    "s_waitcnt vmcnt(0)\n\t" \
    "v_mad_u32_u16 v40, v52, 1, v3\n\t" \
    "v_mad_u32_u16 v41, v52, 1, v3 op_sel:[1,0,0,0]\n\t" \
    "ds_write_addtid_b32 v218 offset:10080\n\t" \
    "v_mad_u32_u16 v42, v53, 1, v3\n\t" \
    "v_mad_u32_u16 v43, v53, 1, v3 op_sel:[1,0,0,0]\n\t" \
    "ds_write_addtid_b32 v219 offset:10368\n\t" \
    "v_mad_u32_u16 v44, v54, 1, v3\n\t" \
    "v_mad_u32_u16 v45, v54, 1, v3 op_sel:[1,0,0,0]\n\t" \
    "ds_write_addtid_b32 v220 offset:10656\n\t" \
    "v_mad_u32_u16 v46, v55, 1, v3\n\t" \
    "v_mad_u32_u16 v47, v55, 1, v3 op_sel:[1,0,0,0]\n\t" \
    "ds_write_addtid_b32 v221 offset:10944\n\t" \
    "v_mad_u32_u16 v48, v56, 1, v3\n\t" \
    "v_mad_u32_u16 v49, v56, 1, v3 op_sel:[1,0,0,0]\n\t" \
    "ds_write_addtid_b32 v222 offset:11232\n\t" \
    "v_mad_u32_u16 v50, v57, 1, v3\n\t" \
    "ds_write_addtid_b32 v223 offset:11520\n\t" \
    "s_waitcnt lgkmcnt(0)\n\t" \
    "ds_read_b128 v[96:99], v58 offset:11808\n\t" \
    "ds_read_b128 v[112:115], v58 offset:0\n\t" \
    "ds_read_b128 v[100:103], v58 offset:11872\n\t" \
    "ds_read_b128 v[116:119], v58 offset:64\n\t" \
    "ds_read_b128 v[104:107], v58 offset:11936\n\t" \
    "ds_read_b128 v[120:123], v58 offset:128\n\t" \
    "ds_read_b128 v[108:111], v58 offset:12000\n\t" \
    "ds_read_b128 v[124:127], v58 offset:192\n\t" \
    "ds_read_b128 v[128:131], v58 offset:4608\n\t" \
    "ds_read_b128 v[132:135], v58 offset:4672\n\t" \
    "ds_read_b128 v[136:139], v58 offset:4736\n\t" \
    "ds_read_b128 v[140:143], v58 offset:4800\n\t" \
    "ds_read_b128 v[144:147], v58 offset:9216\n\t" \
    "ds_read_b128 v[148:151], v58 offset:9280\n\t" \
    "ds_read_b128 v[152:155], v58 offset:9344\n\t" \
    "ds_read_b128 v[156:159], v58 offset:9408\n\t" \
    "s_waitcnt lgkmcnt(14)\n\t" \
    "v_mfma_f32_16x16x32_f16 v[160:163], v[112:115], v[96:99], 0\n\t" \
    "s_waitcnt lgkmcnt(12)\n\t" \
    "v_mfma_f32_16x16x32_f16 v[160:163], v[116:119], v[100:103], v[160:163]\n\t" \
    "s_waitcnt lgkmcnt(10)\n\t" \
    "v_mfma_f32_16x16x32_f16 v[160:163], v[120:123], v[104:107], v[160:163]\n\t" \
    "s_waitcnt lgkmcnt(8)\n\t" \
    "v_mfma_f32_16x16x32_f16 v[160:163], v[124:127], v[108:111], v[160:163]\n\t" \
    "s_waitcnt lgkmcnt(7)\n\t" \
    "v_mfma_f32_16x16x32_f16 v[164:167], v[128:131], v[96:99], 0\n\t" \
    "s_waitcnt lgkmcnt(6)\n\t" \
    "v_mfma_f32_16x16x32_f16 v[164:167], v[132:135], v[100:103], v[164:167]\n\t" \
    "s_waitcnt lgkmcnt(5)\n\t" \
    "v_mfma_f32_16x16x32_f16 v[164:167], v[136:139], v[104:107], v[164:167]\n\t" \
    "s_waitcnt lgkmcnt(4)\n\t" \
    "v_mfma_f32_16x16x32_f16 v[164:167], v[140:143], v[108:111], v[164:167]\n\t" \
    "s_waitcnt lgkmcnt(3)\n\t" \
    "v_mfma_f32_16x16x32_f16 v[168:171], v[144:147], v[96:99], 0\n\t" \
    "s_waitcnt lgkmcnt(2)\n\t" \
    "v_mfma_f32_16x16x32_f16 v[168:171], v[148:151], v[100:103], v[168:171]\n\t" \
    "s_waitcnt lgkmcnt(1)\n\t" \
    "v_mfma_f32_16x16x32_f16 v[168:171], v[152:155], v[104:107], v[168:171]\n\t" \
    "s_waitcnt lgkmcnt(0)\n\t" \
    "v_mfma_f32_16x16x32_f16 v[168:171], v[156:159], v[108:111], v[168:171]\n\t" \
    "v_mul_f32 v160, s58, v160\n\t" \
    "v_mul_f32 v161, s58, v161\n\t" \
    "v_mul_f32 v162, s58, v162\n\t" \
    "v_mul_f32 v163, s58, v163\n\t" \
    "ds_write_b128 v59, v[160:163] offset:0\n\t" \
    "v_mul_f32 v164, s58, v164\n\t" \
    "v_mul_f32 v165, s58, v165\n\t" \
    "v_mul_f32 v166, s58, v166\n\t" \
    "v_mul_f32 v167, s58, v167\n\t" \
    "ds_write_b128 v60, v[164:167] offset:0\n\t" \
    "v_mul_f32 v168, s58, v168\n\t" \
    "v_mul_f32 v169, s58, v169\n\t" \
    "v_mul_f32 v170, s58, v170\n\t" \
    "v_mul_f32 v171, s58, v171\n\t" \
    "s_mov_b32 exec_hi, 0\n\t" \
    "ds_write_b64 v61, v[168:169] offset:0\n\t" \
    "ds_write_b64 v61, v[170:171] offset:128\n\t" \
    "s_mov_b32 exec_lo, 0\n\t" \
    "s_mov_b32 exec_hi, 1\n\t" \
    "ds_write_b32 v61, v168 offset:112\n\t" \
    "s_mov_b64 exec, -1\n\t" \
    "v_mov_b32 v2, 1\n\t" \
    "s_waitcnt lgkmcnt(0)\n\t" \
    "ds_write_b32 v63, v2 offset:0\n\t" \
    "s_mov_b32 s59, 0x100000\n\t" \
    ".Lsy7_%=:\n\t" \
    "ds_read_b32 v3, v51 offset:0\n\t" \
    "s_waitcnt lgkmcnt(0)\n\t" \
    "v_readfirstlane_b32 s42, v3\n\t" \
    "s_sub_u32 s59, s59, 1\n\t" \
    "s_cbranch_scc1 .Lsz7_%=\n\t" \
    "s_cmp_lt_u32 s42, 1\n\t" \
    "s_cbranch_scc1 .Lsy7_%=\n\t" \
    ".Lsz7_%=:\n\t" \
    "ds_read2st64_b32 v[96:97], v40 offset1:11\n\t" \
    "ds_read2st64_b32 v[98:99], v41 offset1:11\n\t" \
    "ds_read2st64_b32 v[100:101], v42 offset1:11\n\t" \
    "ds_read2st64_b32 v[102:103], v43 offset1:11\n\t" \
    "ds_read2st64_b32 v[104:105], v44 offset1:11\n\t" \
    "ds_read2st64_b32 v[106:107], v45 offset1:11\n\t" \
    "ds_read2st64_b32 v[108:109], v46 offset1:11\n\t" \
    "ds_read2st64_b32 v[110:111], v47 offset1:11\n\t" \
    "ds_read2st64_b32 v[112:113], v48 offset1:11\n\t" \
    "ds_read2st64_b32 v[114:115], v49 offset1:11\n\t" \
    "ds_read2st64_b32 v[116:117], v50 offset1:11\n\t" \
    "s_waitcnt lgkmcnt(10)\n\t" \
    "v_add_f32 v96, v96, v97\n\t" \
    "s_waitcnt lgkmcnt(9)\n\t" \
    "v_add_f32 v98, v98, v99\n\t" \
    "s_waitcnt lgkmcnt(8)\n\t" \
    "v_add_f32 v100, v100, v101\n\t" \
    "s_waitcnt lgkmcnt(7)\n\t" \
    "v_add_f32 v102, v102, v103\n\t" \
    "s_waitcnt lgkmcnt(6)\n\t" \
    "v_add_f32 v104, v104, v105\n\t" \
    "s_waitcnt lgkmcnt(5)\n\t" \
    "v_add_f32 v106, v106, v107\n\t" \
    "s_waitcnt lgkmcnt(4)\n\t" \
    "v_add_f32 v108, v108, v109\n\t" \
    "s_waitcnt lgkmcnt(3)\n\t" \
    "v_add_f32 v110, v110, v111\n\t" \
    "s_waitcnt lgkmcnt(2)\n\t" \
    "v_add_f32 v112, v112, v113\n\t" \
    "s_waitcnt lgkmcnt(1)\n\t" \
    "v_add_f32 v114, v114, v115\n\t" \
    "s_waitcnt lgkmcnt(0)\n\t" \
    "v_add_f32 v116, v116, v117\n\t" \
    "v_cvt_pk_bf16_f32 v128, v96, v98\n\t" \
    "v_cvt_pk_bf16_f32 v129, v100, v102\n\t" \
    "v_cvt_pk_bf16_f32 v130, v104, v106\n\t" \
    "v_cvt_pk_bf16_f32 v131, v108, v110\n\t" \
    "ds_write_b128 v62, v[128:131] offset:0\n\t" \
    "v_cvt_pk_bf16_f32 v132, v98, v100\n\t" \
    "v_cvt_pk_bf16_f32 v133, v102, v104\n\t" \
    "v_cvt_pk_bf16_f32 v134, v106, v108\n\t" \
    "v_cvt_pk_bf16_f32 v135, v110, v112\n\t" \
    "ds_write_b128 v62, v[132:135] offset:2112\n\t" \
    "v_cvt_pk_bf16_f32 v136, v100, v102\n\t" \
    "v_cvt_pk_bf16_f32 v137, v104, v106\n\t" \
    "v_cvt_pk_bf16_f32 v138, v108, v110\n\t" \
    "v_cvt_pk_bf16_f32 v139, v112, v114\n\t" \
    "ds_write_b128 v62, v[136:139] offset:4224\n\t" \
    "v_cvt_pk_bf16_f32 v140, v102, v104\n\t" \
    "v_cvt_pk_bf16_f32 v141, v106, v108\n\t" \
    "v_cvt_pk_bf16_f32 v142, v110, v112\n\t" \
    "v_cvt_pk_bf16_f32 v143, v114, v116\n\t" \
    "ds_write_b128 v62, v[140:143] offset:6336\n\t" \
    "v_mov_b32 v2, 2\n\t" \
    "s_waitcnt lgkmcnt(0)\n\t" \
    "ds_write_b32 v63, v2 offset:0\n\t" \
    "s_mov_b32 s59, 0x100000\n\t" \
    ".Lsy8_%=:\n\t" \
    "ds_read_b32 v3, v51 offset:0\n\t" \
    "s_waitcnt lgkmcnt(0)\n\t" \
    "v_readfirstlane_b32 s42, v3\n\t" \
    "s_sub_u32 s59, s59, 1\n\t" \
    "s_cbranch_scc1 .Lsz8_%=\n\t" \
    "s_cmp_lt_u32 s42, 2\n\t" \
    "s_cbranch_scc1 .Lsy8_%=\n\t" \
    ".Lsz8_%=:\n\t" \
    "ds_read_b64 v[128:129], v92 offset:704\n\t" \
    "ds_read_b64 v[130:131], v92 offset:712\n\t" \
    "ds_read_b64 v[132:133], v92 offset:192\n\t" \
    "ds_read_b64 v[134:135], v92 offset:200\n\t" \
    "ds_read_b64 v[168:169], v67 offset:1312\n\t" \
    "ds_read_b64 v[170:171], v67 offset:1440\n\t" \
    "ds_read_b64 v[172:173], v67 offset:1280\n\t" \
    "ds_read_b64 v[174:175], v67 offset:1408\n\t" \
    "ds_read_b64 v[176:177], v67 offset:1248\n\t" \
    "ds_read_b64 v[178:179], v67 offset:1376\n\t" \
    "s_lshl_b32 s42, s51, 8\n\t" \
    "v_subrev_u32 v9, s42, v67\n\t" \
    "v_add_u32 v6, 0xfffffea0, v92\n\t" \
    "v_subrev_u32 v8, s42, v93\n\t" \
    "s_lshl_b32 s42, s51, 4\n\t" \
    "v_subrev_u32 v7, s42, v93\n\t" \
    "ds_read_b64 v[136:137], v92 offset:640\n\t" \
    "ds_read_b64 v[138:139], v92 offset:648\n\t" \
    "ds_read_b64 v[140:141], v92 offset:128\n\t" \
    "ds_read_b64 v[142:143], v92 offset:136\n\t" \
    "ds_read_b64 v[180:181], v9 offset:1856\n\t" \
    "ds_read_b64 v[182:183], v9 offset:2016\n\t" \
    "ds_read_b64 v[184:185], v9 offset:1824\n\t" \
    "ds_read_b64 v[186:187], v9 offset:1984\n\t" \
    "ds_read_b64 v[188:189], v9 offset:1792\n\t" \
    "ds_read_b64 v[190:191], v9 offset:1952\n\t" \
    "ds_read_b64 v[192:193], v9 offset:1760\n\t" \
    "ds_read_b64 v[194:195], v9 offset:1920\n\t" \
    "ds_read_b64 v[160:161], v6 offset:32\n\t" \
    "ds_read_b64 v[162:163], v6 offset:40\n\t" \
    "ds_read_b64 v[164:165], v6 offset:1152\n\t" \
    "ds_read_b64 v[166:167], v6 offset:1160\n\t" \
    "ds_read_b64 v[144:145], v92 offset:576\n\t" \
    "ds_read_b64 v[146:147], v92 offset:584\n\t" \
    "ds_read_b64 v[148:149], v92 offset:64\n\t" \
    "ds_read_b64 v[150:151], v92 offset:72\n\t" \
    "ds_read_b64 v[152:153], v92 offset:512\n\t" \
    "ds_read_b64 v[154:155], v92 offset:520\n\t" \
    "ds_read_b64 v[156:157], v92 offset:0\n\t" \
    "ds_read_b64 v[158:159], v92 offset:8\n\t" \
    "v_mov_b32 v2, 0x1b264\n\t" \
    "v_lshlrev_b32 v5, 4, v1\n\t" \
    "v_add_u32 v5, 0x1f280, v5\n\t" \
    "s_mov_b32 s59, 0x100000\n\t" \
    ".Lsy9_%=:\n\t" \
    "ds_read_b32 v3, v2 offset:0\n\t" \
    "s_waitcnt lgkmcnt(0)\n\t" \
    "v_readfirstlane_b32 s42, v3\n\t" \
    "s_sub_u32 s59, s59, 1\n\t" \
    "s_cbranch_scc1 .Lsz9_%=\n\t" \
    "s_cmp_lt_u32 s42, 1\n\t" \
    "s_cbranch_scc1 .Lsy9_%=\n\t" \
    ".Lsz9_%=:\n\t" \
    "ds_read_b128 v[84:87], v5 offset:0\n\t" \
    "ds_read_b128 v[88:91], v5 offset:1024\n\t" \
    "ds_read_b128 v[248:251], v5 offset:2048\n\t" \
    "ds_read_b128 v[252:255], v5 offset:3072\n\t" \
    "v_mov_b32 v2, 0x1b260\n\t" \
    "s_mov_b32 s59, 0x100000\n\t" \
    ".Lsy10_%=:\n\t" \
    "ds_read_b32 v3, v2 offset:0\n\t" \
    "s_waitcnt lgkmcnt(0)\n\t" \
    "v_readfirstlane_b32 s42, v3\n\t" \
    "s_sub_u32 s59, s59, 1\n\t" \
    "s_cbranch_scc1 .Lsz10_%=\n\t" \
    "s_cmp_lt_u32 s42, 2\n\t" \
    "s_cbranch_scc1 .Lsy10_%=\n\t" \
    ".Lsz10_%=:\n\t" \
    "v_mov_b32 v2, 0x1b264\n\t" \
    "s_mov_b32 s59, 0x100000\n\t" \
    ".Lsy11_%=:\n\t" \
    "ds_read_b32 v3, v2 offset:0\n\t" \
    "s_waitcnt lgkmcnt(0)\n\t" \
    "v_readfirstlane_b32 s42, v3\n\t" \
    "s_sub_u32 s59, s59, 1\n\t" \
    "s_cbranch_scc1 .Lsz11_%=\n\t" \
    "s_cmp_lt_u32 s42, 2\n\t" \
    "s_cbranch_scc1 .Lsy11_%=\n\t" \
    ".Lsz11_%=:\n\t" \
    "ds_read_b128 v[16:19], v7 offset:448\n\t" \
    "ds_read_b128 v[20:23], v7 offset:960\n\t" \
    "ds_read_b128 v[24:27], v7 offset:1472\n\t" \
    "ds_read_b128 v[28:31], v7 offset:1984\n\t" \
    "ds_read_b128 v[212:215], v8 offset:448\n\t" \
    "ds_read_b128 v[216:219], v8 offset:960\n\t" \
    "ds_read_b128 v[220:223], v8 offset:1472\n\t" \
    "ds_read_b128 v[224:227], v8 offset:1984\n\t" \
    "s_waitcnt lgkmcnt(7)\n\t" \
    "v_mfma_f32_16x16x32_bf16 v[196:199], v[16:19], v[84:87], 0\n\t" \
    "v_mfma_f32_16x16x32_bf16 v[200:203], v[16:19], v[88:91], 0\n\t" \
    "s_waitcnt lgkmcnt(3)\n\t" \
    "v_mfma_f32_16x16x32_bf16 v[96:99], v[128:131], v[212:215], 0\n\t" \
    "ds_read_b128 v[228:231], v8 offset:384\n\t" \
    "v_mfma_f32_16x16x32_bf16 v[100:103], v[132:135], v[212:215], 0\n\t" \
    "s_waitcnt lgkmcnt(3)\n\t" \
    "v_mfma_f32_16x16x32_bf16 v[100:103], v[128:131], v[216:219], v[100:103]\n\t" \
    "ds_read_b128 v[232:235], v8 offset:896\n\t" \
    "v_mfma_f32_16x16x32_bf16 v[104:107], v[132:135], v[216:219], 0\n\t" \
    "s_waitcnt lgkmcnt(3)\n\t" \
    "v_mfma_f32_16x16x32_bf16 v[104:107], v[128:131], v[220:223], v[104:107]\n\t" \
    "ds_read_b128 v[212:215], v8 offset:1408\n\t" \
    "v_cvt_pk_bf16_f32 v204, v196, v197\n\t" \
    "v_mfma_f32_16x16x32_bf16 v[108:111], v[132:135], v[220:223], 0\n\t" \
    "v_cvt_pk_bf16_f32 v205, v198, v199\n\t" \
    "v_cvt_pk_bf16_f32 v206, v200, v201\n\t" \
    "v_cvt_pk_bf16_f32 v207, v202, v203\n\t" \
    "s_waitcnt lgkmcnt(3)\n\t" \
    "v_mfma_f32_16x16x32_bf16 v[108:111], v[128:131], v[224:227], v[108:111]\n\t" \
    "ds_read_b128 v[216:219], v8 offset:1920\n\t" \
    "v_mfma_f32_16x16x32_bf16 v[112:115], v[132:135], v[224:227], 0\n\t" \
    "v_mfma_f32_16x16x32_bf16 v[104:107], v[204:207], v[168:171], v[104:107]\n\t" \
    "v_mfma_f32_16x16x32_bf16 v[108:111], v[204:207], v[172:175], v[108:111]\n\t" \
    "v_mfma_f32_16x16x32_bf16 v[112:115], v[204:207], v[176:179], v[112:115]\n\t" \
    "v_mfma_f32_16x16x32_bf16 v[196:199], v[20:23], v[84:87], 0\n\t" \
    "v_mfma_f32_16x16x32_bf16 v[200:203], v[20:23], v[88:91], 0\n\t" \
    "s_waitcnt lgkmcnt(3)\n\t" \
    "v_mfma_f32_16x16x32_bf16 v[96:99], v[136:139], v[228:231], v[96:99]\n\t" \
    "ds_read_b128 v[220:223], v8 offset:320\n\t" \
    "v_mfma_f32_16x16x32_bf16 v[100:103], v[140:143], v[228:231], v[100:103]\n\t" \
    "s_waitcnt lgkmcnt(3)\n\t" \
    "v_mfma_f32_16x16x32_bf16 v[100:103], v[136:139], v[232:235], v[100:103]\n\t" \
    "ds_read_b128 v[224:227], v8 offset:832\n\t" \
    "v_mfma_f32_16x16x32_bf16 v[104:107], v[140:143], v[232:235], v[104:107]\n\t" \
    "s_waitcnt lgkmcnt(3)\n\t" \
    "v_mfma_f32_16x16x32_bf16 v[104:107], v[136:139], v[212:215], v[104:107]\n\t" \
    "ds_read_b128 v[228:231], v8 offset:1344\n\t" \
    "v_cvt_pk_bf16_f32 v208, v196, v197\n\t" \
    "v_mfma_f32_16x16x32_bf16 v[108:111], v[140:143], v[212:215], v[108:111]\n\t" \
    "v_cvt_pk_bf16_f32 v209, v198, v199\n\t" \
    "v_cvt_pk_bf16_f32 v210, v200, v201\n\t" \
    "v_cvt_pk_bf16_f32 v211, v202, v203\n\t" \
    "s_waitcnt lgkmcnt(3)\n\t" \
    "v_mfma_f32_16x16x32_bf16 v[108:111], v[136:139], v[216:219], v[108:111]\n\t" \
    "ds_read_b128 v[232:235], v8 offset:1856\n\t" \
    "v_mfma_f32_16x16x32_bf16 v[112:115], v[140:143], v[216:219], v[112:115]\n\t" \
    "v_mfma_f32_16x16x32_bf16 v[108:111], v[208:211], v[168:171], v[108:111]\n\t" \
    "v_mfma_f32_16x16x32_bf16 v[112:115], v[208:211], v[172:175], v[112:115]\n\t" \
    "v_mfma_f32_16x16x32_bf16 v[116:119], v[208:211], v[176:179], 0\n\t" \
    "v_mfma_f32_16x16x32_bf16 v[196:199], v[24:27], v[84:87], 0\n\t" \
    "v_mfma_f32_16x16x32_bf16 v[200:203], v[24:27], v[88:91], 0\n\t" \
    "s_waitcnt lgkmcnt(3)\n\t" \
    "v_mfma_f32_16x16x32_bf16 v[96:99], v[144:147], v[220:223], v[96:99]\n\t" \
    "ds_read_b128 v[212:215], v8 offset:256\n\t" \
    "v_mfma_f32_16x16x32_bf16 v[100:103], v[148:151], v[220:223], v[100:103]\n\t" \
    "s_waitcnt lgkmcnt(3)\n\t" \
    "v_mfma_f32_16x16x32_bf16 v[100:103], v[144:147], v[224:227], v[100:103]\n\t" \
    "ds_read_b128 v[216:219], v8 offset:768\n\t" \
    "v_mfma_f32_16x16x32_bf16 v[104:107], v[148:151], v[224:227], v[104:107]\n\t" \
    "s_waitcnt lgkmcnt(3)\n\t" \
    "v_mfma_f32_16x16x32_bf16 v[104:107], v[144:147], v[228:231], v[104:107]\n\t" \
    "ds_read_b128 v[220:223], v8 offset:1280\n\t" \
    "v_cvt_pk_bf16_f32 v204, v196, v197\n\t" \
    "v_mfma_f32_16x16x32_bf16 v[108:111], v[148:151], v[228:231], v[108:111]\n\t" \
    "v_cvt_pk_bf16_f32 v205, v198, v199\n\t" \
    "v_cvt_pk_bf16_f32 v206, v200, v201\n\t" \
    "v_cvt_pk_bf16_f32 v207, v202, v203\n\t" \
    "s_waitcnt lgkmcnt(3)\n\t" \
    "v_mfma_f32_16x16x32_bf16 v[108:111], v[144:147], v[232:235], v[108:111]\n\t" \
    "ds_read_b128 v[224:227], v8 offset:1792\n\t" \
    "v_mfma_f32_16x16x32_bf16 v[112:115], v[148:151], v[232:235], v[112:115]\n\t" \
    "v_mfma_f32_16x16x32_bf16 v[112:115], v[204:207], v[168:171], v[112:115]\n\t" \
    "v_mfma_f32_16x16x32_bf16 v[116:119], v[204:207], v[172:175], v[116:119]\n\t" \
    "v_mfma_f32_16x16x32_bf16 v[120:123], v[204:207], v[176:179], 0\n\t" \
    "v_mfma_f32_16x16x32_bf16 v[196:199], v[28:31], v[84:87], 0\n\t" \
    "v_mfma_f32_16x16x32_bf16 v[200:203], v[28:31], v[88:91], 0\n\t" \
    "s_waitcnt lgkmcnt(3)\n\t" \
    "v_mfma_f32_16x16x32_bf16 v[96:99], v[152:155], v[212:215], v[96:99]\n\t" \
    "v_mfma_f32_16x16x32_bf16 v[100:103], v[156:159], v[212:215], v[100:103]\n\t" \
    "s_waitcnt lgkmcnt(2)\n\t" \
    "v_mfma_f32_16x16x32_bf16 v[100:103], v[152:155], v[216:219], v[100:103]\n\t" \
    "v_mfma_f32_16x16x32_bf16 v[104:107], v[156:159], v[216:219], v[104:107]\n\t" \
    "s_waitcnt lgkmcnt(1)\n\t" \
    "v_mfma_f32_16x16x32_bf16 v[104:107], v[152:155], v[220:223], v[104:107]\n\t" \
    "v_cvt_pk_bf16_f32 v208, v196, v197\n\t" \
    "v_mfma_f32_16x16x32_bf16 v[108:111], v[156:159], v[220:223], v[108:111]\n\t" \
    "v_cvt_pk_bf16_f32 v209, v198, v199\n\t" \
    "v_cvt_pk_bf16_f32 v210, v200, v201\n\t" \
    "v_cvt_pk_bf16_f32 v211, v202, v203\n\t" \
    "s_waitcnt lgkmcnt(0)\n\t" \
    "v_mfma_f32_16x16x32_bf16 v[108:111], v[152:155], v[224:227], v[108:111]\n\t" \
    "v_mfma_f32_16x16x32_bf16 v[112:115], v[156:159], v[224:227], v[112:115]\n\t" \
    "v_mfma_f32_16x16x32_bf16 v[116:119], v[208:211], v[168:171], v[116:119]\n\t" \
    "v_mfma_f32_16x16x32_bf16 v[120:123], v[208:211], v[172:175], v[120:123]\n\t" \
    "v_mfma_f32_16x16x32_bf16 v[124:127], v[208:211], v[176:179], 0\n\t" \
    "s_cmp_eq_u32 s51, 0\n\t" \
    "s_cbranch_scc0 .Lh1_%=\n\t" \
    "ds_read_b128 v[236:239], v93 offset:1456\n\t" \
    "v_mfma_f32_16x16x32_bf16 v[104:107], v[160:163], v[16:19], v[104:107]\n\t" \
    "v_mfma_f32_16x16x32_bf16 v[108:111], v[160:163], v[20:23], v[108:111]\n\t" \
    "ds_write_b128 v94, v[96:99] offset:0\n\t" \
    "v_mfma_f32_16x16x32_bf16 v[112:115], v[160:163], v[24:27], v[112:115]\n\t" \
    "v_mfma_f32_16x16x32_bf16 v[116:119], v[160:163], v[28:31], v[116:119]\n\t" \
    "ds_write_b128 v94, v[100:103] offset:1280\n\t" \
    "s_waitcnt lgkmcnt(2)\n\t" \
    "v_mfma_f32_16x16x32_bf16 v[196:199], v[24:27], v[248:251], 0\n\t" \
    "v_mfma_f32_16x16x32_bf16 v[200:203], v[236:239], v[252:255], 0\n\t" \
    "ds_write_b128 v94, v[104:107] offset:2560\n\t" \
    "v_mfma_f32_16x16x32_bf16 v[112:115], v[164:167], v[16:19], v[112:115]\n\t" \
    "ds_write_b128 v94, v[108:111] offset:3840\n\t" \
    "s_barrier\n\t" \
    "ds_read_b128 v[32:35], v7 offset:2496\n\t" \
    "ds_read_b128 v[36:39], v7 offset:3008\n\t" \
    "ds_read_b128 v[228:231], v8 offset:2496\n\t" \
    "ds_read_b128 v[232:235], v8 offset:3008\n\t" \
    "ds_read_b128 v[212:215], v8 offset:3520\n\t" \
    "ds_read_b128 v[216:219], v8 offset:4032\n\t" \
    "v_mfma_f32_16x16x32_bf16 v[116:119], v[164:167], v[20:23], v[116:119]\n\t" \
    "v_cvt_pk_bf16_f32 v204, v196, v197\n\t" \
    "v_cvt_pk_bf16_f32 v205, v198, v199\n\t" \
    "v_mfma_f32_16x16x32_bf16 v[120:123], v[164:167], v[24:27], v[120:123]\n\t" \
    "v_cvt_pk_bf16_f32 v206, v200, v201\n\t" \
    "v_cvt_pk_bf16_f32 v207, v202, v203\n\t" \
    "v_mfma_f32_16x16x32_bf16 v[124:127], v[164:167], v[28:31], v[124:127]\n\t" \
    "s_nop 0\n\t" \
    "v_mfma_f32_16x16x32_bf16 v[120:123], v[204:207], v[180:183], v[120:123]\n\t" \
    "v_mfma_f32_16x16x32_bf16 v[124:127], v[204:207], v[184:187], v[124:127]\n\t" \
    "s_branch .Lh2_%=\n\t" \
    ".Lh1_%=:\n\t" \
    "ds_read_b128 v[236:239], v93 offset:448\n\t" \
    "ds_read_b128 v[240:243], v93 offset:960\n\t" \
    "ds_read_b128 v[244:247], v93 offset:1984\n\t" \
    "ds_write_b128 v94, v[96:99] offset:0\n\t" \
    "s_waitcnt lgkmcnt(3)\n\t" \
    "v_mfma_f32_16x16x32_bf16 v[196:199], v[236:239], v[248:251], 0\n\t" \
    "v_mfma_f32_16x16x32_bf16 v[200:203], v[16:19], v[252:255], 0\n\t" \
    "ds_write_b128 v94, v[100:103] offset:1280\n\t" \
    "v_mov_b32 v160, 0\n\t" \
    "v_mov_b32 v161, 0\n\t" \
    "v_mov_b32 v162, 0\n\t" \
    "v_mov_b32 v163, 0\n\t" \
    "s_nop 1\n\t" \
    "v_cvt_pk_bf16_f32 v204, v196, v197\n\t" \
    "v_cvt_pk_bf16_f32 v205, v198, v199\n\t" \
    "v_cvt_pk_bf16_f32 v206, v200, v201\n\t" \
    "v_cvt_pk_bf16_f32 v207, v202, v203\n\t" \
    "s_waitcnt lgkmcnt(3)\n\t" \
    "v_mfma_f32_16x16x32_bf16 v[196:199], v[240:243], v[248:251], 0\n\t" \
    "v_mfma_f32_16x16x32_bf16 v[200:203], v[20:23], v[252:255], 0\n\t" \
    "ds_write_b128 v94, v[104:107] offset:2560\n\t" \
    "v_mfma_f32_16x16x32_bf16 v[112:115], v[204:207], v[180:183], v[112:115]\n\t" \
    "v_mfma_f32_16x16x32_bf16 v[116:119], v[204:207], v[184:187], v[116:119]\n\t" \
    "ds_write_b128 v94, v[108:111] offset:3840\n\t" \
    "v_mfma_f32_16x16x32_bf16 v[120:123], v[204:207], v[188:191], v[120:123]\n\t" \
    "s_nop 1\n\t" \
    "v_cvt_pk_bf16_f32 v208, v196, v197\n\t" \
    "v_cvt_pk_bf16_f32 v209, v198, v199\n\t" \
    "v_mfma_f32_16x16x32_bf16 v[124:127], v[204:207], v[192:195], v[124:127]\n\t" \
    "v_cvt_pk_bf16_f32 v210, v200, v201\n\t" \
    "v_cvt_pk_bf16_f32 v211, v202, v203\n\t" \
    "s_waitcnt lgkmcnt(4)\n\t" \
    "v_mfma_f32_16x16x32_bf16 v[196:199], v[244:247], v[248:251], 0\n\t" \
    "v_mfma_f32_16x16x32_bf16 v[200:203], v[28:31], v[252:255], 0\n\t" \
    "s_barrier\n\t" \
    "ds_read_b128 v[32:35], v7 offset:2496\n\t" \
    "ds_read_b128 v[36:39], v7 offset:3008\n\t" \
    "ds_read_b128 v[228:231], v8 offset:2496\n\t" \
    "ds_read_b128 v[232:235], v8 offset:3008\n\t" \
    "ds_read_b128 v[212:215], v8 offset:3520\n\t" \
    "ds_read_b128 v[216:219], v8 offset:4032\n\t" \
    "v_mfma_f32_16x16x32_bf16 v[116:119], v[208:211], v[180:183], v[116:119]\n\t" \
    "v_mfma_f32_16x16x32_bf16 v[120:123], v[208:211], v[184:187], v[120:123]\n\t" \
    "v_cvt_pk_bf16_f32 v204, v196, v197\n\t" \
    "v_cvt_pk_bf16_f32 v205, v198, v199\n\t" \
    "v_mfma_f32_16x16x32_bf16 v[124:127], v[208:211], v[188:191], v[124:127]\n\t" \
    "v_cvt_pk_bf16_f32 v206, v200, v201\n\t" \
    "v_cvt_pk_bf16_f32 v207, v202, v203\n\t" \
    "s_nop 1\n\t" \
    "v_mfma_f32_16x16x32_bf16 v[124:127], v[204:207], v[180:183], v[124:127]\n\t" \
    ".Lh2_%=:\n\t" \
    "s_nop 1\n\t" \
    "s_waitcnt lgkmcnt(5)\n\t" \
    "v_mfma_f32_16x16x32_bf16 v[196:199], v[32:35], v[84:87], 0\n\t" \
    "v_mfma_f32_16x16x32_bf16 v[200:203], v[32:35], v[88:91], 0\n\t" \
    "s_waitcnt lgkmcnt(3)\n\t" \
    "v_mfma_f32_16x16x32_bf16 v[112:115], v[128:131], v[228:231], v[112:115]\n\t" \
    "ds_read_b128 v[220:223], v8 offset:2432\n\t" \
    "v_mfma_f32_16x16x32_bf16 v[116:119], v[132:135], v[228:231], v[116:119]\n\t" \
    "s_waitcnt lgkmcnt(3)\n\t" \
    "v_mfma_f32_16x16x32_bf16 v[116:119], v[128:131], v[232:235], v[116:119]\n\t" \
    "ds_read_b128 v[224:227], v8 offset:2944\n\t" \
    "v_mfma_f32_16x16x32_bf16 v[120:123], v[132:135], v[232:235], v[120:123]\n\t" \
    "s_waitcnt lgkmcnt(3)\n\t" \
    "v_mfma_f32_16x16x32_bf16 v[120:123], v[128:131], v[212:215], v[120:123]\n\t" \
    "ds_read_b128 v[228:231], v8 offset:3456\n\t" \
    "v_cvt_pk_bf16_f32 v204, v196, v197\n\t" \
    "v_mfma_f32_16x16x32_bf16 v[124:127], v[132:135], v[212:215], v[124:127]\n\t" \
    "v_cvt_pk_bf16_f32 v205, v198, v199\n\t" \
    "v_cvt_pk_bf16_f32 v206, v200, v201\n\t" \
    "v_cvt_pk_bf16_f32 v207, v202, v203\n\t" \
    "s_waitcnt lgkmcnt(3)\n\t" \
    "v_mfma_f32_16x16x32_bf16 v[124:127], v[128:131], v[216:219], v[124:127]\n\t" \
    "ds_read_b128 v[232:235], v8 offset:3968\n\t" \
    "v_mfma_f32_16x16x32_bf16 v[120:123], v[204:207], v[168:171], v[120:123]\n\t" \
    "v_mfma_f32_16x16x32_bf16 v[124:127], v[204:207], v[172:175], v[124:127]\n\t" \
    "v_mfma_f32_16x16x32_bf16 v[120:123], v[160:163], v[32:35], v[120:123]\n\t" \
    "v_mfma_f32_16x16x32_bf16 v[196:199], v[36:39], v[84:87], 0\n\t" \
    "v_mfma_f32_16x16x32_bf16 v[200:203], v[36:39], v[88:91], 0\n\t" \
    "s_waitcnt lgkmcnt(3)\n\t" \
    "v_mfma_f32_16x16x32_bf16 v[112:115], v[136:139], v[220:223], v[112:115]\n\t" \
    "ds_read_b128 v[212:215], v8 offset:2368\n\t" \
    "v_mfma_f32_16x16x32_bf16 v[116:119], v[140:143], v[220:223], v[116:119]\n\t" \
    "s_waitcnt lgkmcnt(3)\n\t" \
    "v_mfma_f32_16x16x32_bf16 v[116:119], v[136:139], v[224:227], v[116:119]\n\t" \
    "ds_read_b128 v[216:219], v8 offset:2880\n\t" \
    "v_mfma_f32_16x16x32_bf16 v[120:123], v[140:143], v[224:227], v[120:123]\n\t" \
    "s_waitcnt lgkmcnt(3)\n\t" \
    "v_mfma_f32_16x16x32_bf16 v[120:123], v[136:139], v[228:231], v[120:123]\n\t" \
    "ds_read_b128 v[220:223], v8 offset:3392\n\t" \
    "v_cvt_pk_bf16_f32 v208, v196, v197\n\t" \
    "v_mfma_f32_16x16x32_bf16 v[124:127], v[140:143], v[228:231], v[124:127]\n\t" \
    "v_cvt_pk_bf16_f32 v209, v198, v199\n\t" \
    "v_cvt_pk_bf16_f32 v210, v200, v201\n\t" \
    "v_cvt_pk_bf16_f32 v211, v202, v203\n\t" \
    "s_waitcnt lgkmcnt(3)\n\t" \
    "v_mfma_f32_16x16x32_bf16 v[124:127], v[136:139], v[232:235], v[124:127]\n\t" \
    "ds_read_b128 v[224:227], v8 offset:3904\n\t" \
    "v_mfma_f32_16x16x32_bf16 v[124:127], v[208:211], v[168:171], v[124:127]\n\t" \
    "v_mfma_f32_16x16x32_bf16 v[124:127], v[160:163], v[36:39], v[124:127]\n\t" \
    "s_waitcnt lgkmcnt(3)\n\t" \
    "v_mfma_f32_16x16x32_bf16 v[112:115], v[144:147], v[212:215], v[112:115]\n\t" \
    "ds_read_b128 v[228:231], v8 offset:2304\n\t" \
    "v_mfma_f32_16x16x32_bf16 v[116:119], v[148:151], v[212:215], v[116:119]\n\t" \
    "v_lshlrev_b32 v4, 4, v0\n\t" \
    "v_add_u32 v3, 0x19240, v4\n\t" \
    "s_waitcnt lgkmcnt(3)\n\t" \
    "v_mfma_f32_16x16x32_bf16 v[116:119], v[144:147], v[216:219], v[116:119]\n\t" \
    "ds_read_b128 v[232:235], v8 offset:2816\n\t" \
    "v_mfma_f32_16x16x32_bf16 v[120:123], v[148:151], v[216:219], v[120:123]\n\t" \
    "v_lshlrev_b32 v5, 3, v0\n\t" \
    "v_add_u32 v5, 0x14460, v5\n\t" \
    "s_waitcnt lgkmcnt(3)\n\t" \
    "v_mfma_f32_16x16x32_bf16 v[120:123], v[144:147], v[220:223], v[120:123]\n\t" \
    "ds_read_b128 v[212:215], v8 offset:3328\n\t" \
    "v_mfma_f32_16x16x32_bf16 v[124:127], v[148:151], v[220:223], v[124:127]\n\t" \
    "ds_read_b128 v[60:63], v3 offset:0\n\t" \
    "ds_read_b64 v[64:65], v5 offset:0\n\t" \
    "s_waitcnt lgkmcnt(5)\n\t" \
    "v_mfma_f32_16x16x32_bf16 v[124:127], v[144:147], v[224:227], v[124:127]\n\t" \
    "ds_read_b128 v[216:219], v8 offset:3840\n\t" \
    "ds_read_b64 v[66:67], v5 offset:4576\n\t" \
    "v_and_b32 v6, 3, v0\n\t" \
    "s_waitcnt lgkmcnt(6)\n\t" \
    "v_mfma_f32_16x16x32_bf16 v[112:115], v[152:155], v[228:231], v[112:115]\n\t" \
    "v_mfma_f32_16x16x32_bf16 v[116:119], v[156:159], v[228:231], v[116:119]\n\t" \
    "v_mul_u32_u24 v2, 0x140, v6\n\t" \
    "v_bfe_u32 v6, v0, 2, 4\n\t" \
    "v_lshl_add_u32 v2, v6, 4, v2\n\t" \
    "s_mul_i32 s42, %1, 1280\n\t" \
    "s_waitcnt lgkmcnt(5)\n\t" \
    "v_mfma_f32_16x16x32_bf16 v[116:119], v[152:155], v[232:235], v[116:119]\n\t" \
    "v_mfma_f32_16x16x32_bf16 v[120:123], v[156:159], v[232:235], v[120:123]\n\t" \
    "s_add_u32 s42, s42, 0\n\t" \
    "v_add_u32 v2, s42, v2\n\t" \
    "s_lshl_b32 s42, s44, 13\n\t" \
    "s_mov_b64 s[40:41], %14\n\t" \
    "s_waitcnt lgkmcnt(4)\n\t" \
    "v_mfma_f32_16x16x32_bf16 v[120:123], v[152:155], v[212:215], v[120:123]\n\t" \
    "v_mfma_f32_16x16x32_bf16 v[124:127], v[156:159], v[212:215], v[124:127]\n\t" \
    "s_add_u32 s40, s40, s42\n\t" \
    "s_addc_u32 s41, s41, 0\n\t" \
    "s_lshl_b32 s42, s49, 16\n\t" \
    "s_and_b32 s43, s49, 0xffff0000\n\t" \
    "ds_write_b128 v94, v[112:115] offset:5120\n\t" \
    "s_waitcnt lgkmcnt(2)\n\t" \
    "v_mfma_f32_16x16x32_bf16 v[124:127], v[152:155], v[216:219], v[124:127]\n\t" \
    "s_waitcnt lgkmcnt(1)\n\t" \
    "v_lshlrev_b32 v52, 16, v60\n\t" \
    "v_and_b32 v53, 0xffff0000, v60\n\t" \
    "v_lshlrev_b32 v54, 16, v61\n\t" \
    "ds_write_b128 v94, v[116:119] offset:6400\n\t" \
    "v_and_b32 v55, 0xffff0000, v61\n\t" \
    "v_lshlrev_b32 v56, 16, v62\n\t" \
    "v_and_b32 v57, 0xffff0000, v62\n\t" \
    "v_lshlrev_b32 v58, 16, v63\n\t" \
    "v_and_b32 v59, 0xffff0000, v63\n\t" \
    "v_lshlrev_b32 v60, 16, v64\n\t" \
    "v_and_b32 v61, 0xffff0000, v64\n\t" \
    "v_fmac_f32 v52, s42, v60\n\t" \
    "v_fmac_f32 v54, s42, v61\n\t" \
    "v_lshlrev_b32 v60, 16, v65\n\t" \
    "v_and_b32 v61, 0xffff0000, v65\n\t" \
    "v_fmac_f32 v56, s42, v60\n\t" \
    "v_fmac_f32 v58, s42, v61\n\t" \
    "v_lshlrev_b32 v60, 16, v66\n\t" \
    "v_and_b32 v61, 0xffff0000, v66\n\t" \
    "v_fmac_f32 v53, s43, v60\n\t" \
    "v_fmac_f32 v55, s43, v61\n\t" \
    "v_lshlrev_b32 v60, 16, v67\n\t" \
    "v_and_b32 v61, 0xffff0000, v67\n\t" \
    "v_fmac_f32 v57, s43, v60\n\t" \
    "v_fmac_f32 v59, s43, v61\n\t" \
    "ds_write_b128 v94, v[120:123] offset:7680\n\t" \
    "ds_write_b128 v94, v[124:127] offset:8960\n\t" \
    "s_waitcnt lgkmcnt(0)\n\t" \
    "s_branch .Lepi_%=\n\t" \
    ".Lh1b_%=:\n\t" \
    "s_lshl_b32 s42, s44, 2\n\t" \
    "s_load_dword s49, %10, s42\n\t" \
    "s_barrier\n\t" \
    "s_add_u32 s42, %15, s47\n\t" \
    "s_cmp_lg_u32 s42, 0\n\t" \
    "s_cbranch_scc1 .Lnoep_%=\n\t" \
    "s_mov_b64 s[40:41], %14\n\t" \
    "s_add_u32 s40, s40, 0x620000\n\t" \
    "s_addc_u32 s41, s41, 0\n\t" \
    "s_add_u32 s48, s48, 1\n\t" \
    "v_mov_b32 v10, 0\n\t" \
    "v_mov_b32 v11, s48\n\t" \
    "global_store_dword v10, v11, s[40:41] sc1\n\t" \
    ".Lnoep_%=:\n\t" \
    "s_cmp_ge_u32 %1, 6\n\t" \
    "s_cbranch_scc1 .Lnp_%=\n\t" \
    "v_mov_b32 v12, -1\n\t" \
    "v_mov_b32 v13, -1\n\t" \
    "v_mov_b32 v14, -1\n\t" \
    "v_mov_b32 v15, -1\n\t" \
    "s_mov_b64 s[40:41], %14\n\t" \
    "s_add_u32 s40, s40, 0x800000\n\t" \
    "s_addc_u32 s41, s41, 0\n\t" \
    "s_mul_i32 s42, s44, 2\n\t" \
    "s_add_u32 s42, s42, s47\n\t" \
    "s_mul_i32 s42, s42, 14336\n\t" \
    "s_add_u32 s40, s40, s42\n\t" \
    "s_addc_u32 s41, s41, 0\n\t" \
    "global_store_dwordx4 v32, v[12:15], s[40:41] sc1\n\t" \
    "global_store_dwordx4 v32, v[12:15], s[40:41] offset:1024 sc1\n\t" \
    "global_store_dwordx4 v32, v[12:15], s[40:41] offset:2048 sc1\n\t" \
    "global_store_dwordx4 v32, v[12:15], s[40:41] offset:3072 sc1\n\t" \
    "s_add_u32 s40, s40, 0x1000\n\t" \
    "s_addc_u32 s41, s41, 0\n\t" \
    "global_store_dwordx4 v32, v[12:15], s[40:41] sc1\n\t" \
    "global_store_dwordx4 v32, v[12:15], s[40:41] offset:1024 sc1\n\t" \
    "global_store_dwordx4 v32, v[12:15], s[40:41] offset:2048 sc1\n\t" \
    "global_store_dwordx4 v32, v[12:15], s[40:41] offset:3072 sc1\n\t" \
    "s_add_u32 s40, s40, 0x1000\n\t" \
    "s_addc_u32 s41, s41, 0\n\t" \
    "global_store_dwordx4 v32, v[12:15], s[40:41] sc1\n\t" \
    "global_store_dwordx4 v32, v[12:15], s[40:41] offset:1024 sc1\n\t" \
    "global_store_dwordx4 v32, v[12:15], s[40:41] offset:2048 sc1\n\t" \
    "global_store_dwordx4 v32, v[12:15], s[40:41] offset:3072 sc1\n\t" \
    "s_add_u32 s40, s40, 0x1000\n\t" \
    "s_addc_u32 s41, s41, 0\n\t" \
    "global_store_dwordx4 v32, v[12:15], s[40:41] sc1\n\t" \
    "global_store_dwordx4 v32, v[12:15], s[40:41] offset:1024 sc1\n\t" \
    "s_mul_i32 s42, %15, 2\n\t" \
    "s_add_u32 s42, s42, s47\n\t" \
    "s_mul_i32 s42, s42, 256\n\t" \
    "s_add_u32 s42, s42, 0x650000\n\t" \
    "s_mov_b64 s[40:41], %14\n\t" \
    "s_add_u32 s40, s40, s42\n\t" \
    "s_addc_u32 s41, s41, 0\n\t" \
    "s_mov_b64 exec, 0xffff\n\t" \
    "global_store_dwordx4 v32, v[12:15], s[40:41] sc1\n\t" \
    "s_mov_b64 exec, -1\n\t" \
    ".Lnp_%=:\n\t" \
    "s_waitcnt lgkmcnt(0)\n\t" \
    "v_lshlrev_b32 v4, 4, v0\n\t" \
    "v_add_u32 v3, 0x19240, v4\n\t" \
    "v_lshlrev_b32 v5, 3, v0\n\t" \
    "v_add_u32 v5, 0x14460, v5\n\t" \
    "ds_read_b128 v[60:63], v3 offset:0\n\t" \
    "ds_read_b64 v[64:65], v5 offset:0\n\t" \
    "ds_read_b64 v[66:67], v5 offset:4576\n\t" \
    "v_and_b32 v6, 3, v0\n\t" \
    "v_mul_u32_u24 v2, 0x140, v6\n\t" \
    "v_bfe_u32 v6, v0, 2, 4\n\t" \
    "v_lshl_add_u32 v2, v6, 4, v2\n\t" \
    "s_mul_i32 s42, %1, 1280\n\t" \
    "s_add_u32 s42, s42, 0\n\t" \
    "v_add_u32 v2, s42, v2\n\t" \
    "s_lshl_b32 s42, s44, 13\n\t" \
    "s_mov_b64 s[40:41], %14\n\t" \
    "s_add_u32 s40, s40, s42\n\t" \
    "s_addc_u32 s41, s41, 0\n\t" \
    "s_lshl_b32 s42, s49, 16\n\t" \
    "s_and_b32 s43, s49, 0xffff0000\n\t" \
    "s_waitcnt lgkmcnt(0)\n\t" \
    "v_lshlrev_b32 v52, 16, v60\n\t" \
    "v_and_b32 v53, 0xffff0000, v60\n\t" \
    "v_lshlrev_b32 v54, 16, v61\n\t" \
    "v_and_b32 v55, 0xffff0000, v61\n\t" \
    "v_lshlrev_b32 v56, 16, v62\n\t" \
    "v_and_b32 v57, 0xffff0000, v62\n\t" \
    "v_lshlrev_b32 v58, 16, v63\n\t" \
    "v_and_b32 v59, 0xffff0000, v63\n\t" \
    "v_lshlrev_b32 v60, 16, v64\n\t" \
    "v_and_b32 v61, 0xffff0000, v64\n\t" \
    "v_fmac_f32 v52, s42, v60\n\t" \
    "v_fmac_f32 v54, s42, v61\n\t" \
    "v_lshlrev_b32 v60, 16, v65\n\t" \
    "v_and_b32 v61, 0xffff0000, v65\n\t" \
    "v_fmac_f32 v56, s42, v60\n\t" \
    "v_fmac_f32 v58, s42, v61\n\t" \
    "v_lshlrev_b32 v60, 16, v66\n\t" \
    "v_and_b32 v61, 0xffff0000, v66\n\t" \
    "v_fmac_f32 v53, s43, v60\n\t" \
    "v_fmac_f32 v55, s43, v61\n\t" \
    "v_lshlrev_b32 v60, 16, v67\n\t" \
    "v_and_b32 v61, 0xffff0000, v67\n\t" \
    "v_fmac_f32 v57, s43, v60\n\t" \
    "v_fmac_f32 v59, s43, v61\n\t" \
    ".Lepi_%=:\n\t" \
    "s_barrier\n\t" \
    "ds_read_b128 v[8:11], v2 offset:0\n\t" \
    "ds_read_b128 v[12:15], v2 offset:10240\n\t" \
    "ds_read_b128 v[16:19], v2 offset:20480\n\t" \
    "ds_read_b128 v[20:23], v2 offset:30720\n\t" \
    "s_waitcnt lgkmcnt(2)\n\t" \
    "v_add_f32 v8, v8, v12\n\t" \
    "v_add_f32 v9, v9, v13\n\t" \
    "v_add_f32 v10, v10, v14\n\t" \
    "v_add_f32 v11, v11, v15\n\t" \
    "v_add_f32 v52, v52, v8\n\t" \
    "v_add_f32 v54, v54, v9\n\t" \
    "v_add_f32 v56, v56, v10\n\t" \
    "v_add_f32 v58, v58, v11\n\t" \
    "s_waitcnt lgkmcnt(0)\n\t" \
    "v_add_f32 v16, v16, v20\n\t" \
    "v_add_f32 v17, v17, v21\n\t" \
    "v_add_f32 v18, v18, v22\n\t" \
    "v_add_f32 v19, v19, v23\n\t" \
    "v_add_f32 v53, v53, v16\n\t" \
    "v_add_f32 v55, v55, v17\n\t" \
    "v_add_f32 v57, v57, v18\n\t" \
    "v_add_f32 v59, v59, v19\n\t" \
    "v_cvt_pk_bf16_f32 v8, v52, v53\n\t" \
    "v_cvt_pk_bf16_f32 v9, v54, v55\n\t" \
    "v_cvt_pk_bf16_f32 v10, v56, v57\n\t" \
    "v_cvt_pk_bf16_f32 v11, v58, v59\n\t" \
    "global_store_dwordx4 v4, v[8:11], s[40:41] sc1\n\t" \
    "s_waitcnt vmcnt(0)\n\t" \
    ".Lend_%=:\n\t"

#define SSM_VCLOB "v1", "v2", "v3", "v4", "v5", "v6", "v7", "v8", "v9", "v10", "v11", "v12", "v13", "v14", "v15", "v16", "v17", "v18", "v19", "v20", "v21", "v22", "v23", "v24", "v25", "v26", "v27", "v28", "v29", "v30", "v31", "v32", "v33", "v34", "v35", "v36", "v37", "v38", "v39", "v40", "v41", "v42", "v43", "v44", "v45", "v46", "v47", "v48", "v49", "v50", "v51", "v52", "v53", "v54", "v55", "v56", "v57", "v58", "v59", "v60", "v61", "v62", "v63", "v64", "v65", "v66", "v67", "v68", "v69", "v70", "v71", "v72", "v73", "v74", "v75", "v76", "v77", "v78", "v79", "v80", "v81", "v82", "v83", "v84", "v85", "v86", "v87", "v88", "v89", "v90", "v91", "v92", "v93", "v94", "v95", "v96", "v97", "v98", "v99", "v100", "v101", "v102", "v103", "v104", "v105", "v106", "v107", "v108", "v109", "v110", "v111", "v112", "v113", "v114", "v115", "v116", "v117", "v118", "v119", "v120", "v121", "v122", "v123", "v124", "v125", "v126", "v127", "v128", "v129", "v130", "v131", "v132", "v133", "v134", "v135", "v136", "v137", "v138", "v139", "v140", "v141", "v142", "v143", "v144", "v145", "v146", "v147", "v148", "v149", "v150", "v151", "v152", "v153", "v154", "v155", "v156", "v157", "v158", "v159", "v160", "v161", "v162", "v163", "v164", "v165", "v166", "v167", "v168", "v169", "v170", "v171", "v172", "v173", "v174", "v175", "v176", "v177", "v178", "v179", "v180", "v181", "v182", "v183", "v184", "v185", "v186", "v187", "v188", "v189", "v190", "v191", "v192", "v193", "v194", "v195", "v196", "v197", "v198", "v199", "v200", "v201", "v202", "v203", "v204", "v205", "v206", "v207", "v208", "v209", "v210", "v211", "v212", "v213", "v214", "v215", "v216", "v217", "v218", "v219", "v220", "v221", "v222", "v223", "v224", "v225", "v226", "v227", "v228", "v229", "v230", "v231", "v232", "v233", "v234", "v235", "v236", "v237", "v238", "v239", "v240", "v241", "v242", "v243", "v244", "v245", "v246", "v247", "v248", "v249", "v250", "v251", "v252", "v253", "v254", "v255"
#define SSM_SCLOB "s40", "s41", "s42", "s43", "s44", "s45", "s46", "s47", "s48", "s49", "s50", "s51", "s52", "s53", "s54", "s55", "s56", "s57", "s58", "s59", "s60", "s61", "s62", "s63", "s64", "s65", "s66", "s67", "s68", "s69", "s70", "s71"
#define A_LDS 131712
#define ZTAB 2592, 2592, 2592, 2592, 2592, 2592, 2592, 2592, 2592, 2592, 2592, 2592, 2592, 2592, 2592, 2592, 2592, 2592, 2592, 2592, 2592, 2592, 2592, 2592, 2592, 2592, 2592, 2592, 2592, 2592, 1536, 1536, 1536, 1536, 1532, 1528, 1528, 1528, 1524, 1520, 1520, 1520, 1516, 1512, 1512, 1512, 1508, 1504, 1504, 1504, 1500, 1496, 1496, 1496, 1492, 1488, 1488, 1488, 1484, 1480, 1480, 1480, 1476, 1472, 1472, 1472, 1468, 1464, 1464, 1464, 1460, 1456, 1456, 1456, 1452, 1448, 1448, 1448, 1444, 1440, 1440, 1440, 1436, 1432, 1432, 1432, 1428, 1424, 1424, 1424, 1420, 1416, 1416, 1416, 1412, 1408, 1408, 1408, 1404, 1400, 1400, 1400, 1396, 1392, 1392, 1392, 1388, 1384, 1384, 1384, 1380, 1376, 1376, 1376, 1372, 1368, 1368, 1368, 1364, 1360, 1360, 1360, 1356, 1352, 1352, 1352, 1348, 1344, 1344, 1344, 1340, 1336, 1336, 1336, 1332, 1328, 1328, 1328, 1324, 1320, 1320, 1320, 1316, 1312, 1312, 1312, 1308, 1304, 1304, 1304, 1300, 1296, 1296, 1296, 1292, 1288, 1288, 1288, 1284, 1280, 1280, 1280, 1276, 1272, 1272, 1272, 1268, 1264, 1264, 1264, 1260, 1256, 1256, 1256, 1252, 1248, 1248, 1248, 1244, 1240, 1240, 1240, 1236, 1232, 1232, 1232, 1228, 1224, 1224, 1224, 1220, 1216, 1216, 1216, 1212, 1208, 1208, 1208, 1204, 1200, 1200, 1200, 1196, 1192, 1192, 1192, 1188, 1184, 1184, 1184, 1180, 1176, 1176, 1176, 1172, 1168, 1168, 1168, 1164, 1160, 1160, 1160, 1156, 1152, 1152, 1152, 1148, 1144, 1144, 1144, 1140, 1136, 1136, 1136, 1132, 1128, 1128, 1128, 1124, 1120, 1120, 1120, 1116, 1112, 1112, 1112, 1108, 1104, 1104, 1104, 1100, 1096, 1096, 1096, 1092, 1088, 1088, 1088, 1084, 1080, 1080, 1080, 1076, 1072, 1072, 1072, 1068, 1064, 1064, 1064, 1060, 1056, 1056, 1056, 1052, 1048, 1048, 1048, 1044, 1040, 1040, 1040, 1036, 1032, 1032, 1032, 1028, 1024, 1024, 1020, 1016, 1012, 1008, 1004, 1000, 996, 992, 988, 984, 980, 976, 972, 968, 964, 960, 956, 952, 948, 944, 940, 936, 932, 928, 924, 920, 916, 912, 908, 904, 900, 896, 892, 888, 884, 880, 876, 872, 868, 864, 860, 856, 852, 848, 844, 840, 836, 832, 828, 824, 820, 816, 812, 808, 804, 800, 796, 792, 788, 784, 780, 776, 772, 768, 764, 760, 756, 752, 748, 744, 740, 736, 732, 728, 724, 720, 716, 712, 708, 704, 700, 696, 692, 688, 684, 680, 676, 672, 668, 664, 660, 656, 652, 648, 644, 640, 636, 632, 628, 624, 620, 616, 612, 608, 604, 600, 596, 592, 588, 584, 580, 576, 572, 568, 564, 560, 556, 552, 548, 544, 540, 536, 532, 528, 524, 520, 516, 512, 508, 504, 500, 496, 492, 488, 484, 480, 476, 472, 468, 464, 460, 456, 452, 448, 444, 440, 436, 432, 428, 424, 420, 416, 412, 408, 404, 400, 396, 392, 388, 384, 380, 376, 372, 368, 364, 360, 356, 352, 348, 344, 340, 336, 332, 328, 324, 320, 316, 312, 308, 304, 300, 296, 292, 288, 284, 280, 276, 272, 268, 264, 260, 256, 252, 248, 244, 240, 236, 232, 228, 224, 220, 216, 212, 208, 204, 200, 196, 192, 188, 184, 180, 176, 172, 168, 164, 160, 156, 152, 148, 144, 140, 136, 132, 128, 124, 120, 116, 112, 108, 104, 100, 96, 92, 88, 84, 80, 76, 72, 68, 64, 60, 56, 52, 48, 44, 40, 36, 32, 28, 24, 20, 16, 12, 8, 4, 0, 2592, 2592, 2592, 2592, 2592, 2592, 2592, 2592, 2592, 2592, 2592, 2592, 2592, 2592, 2592, 2592, 2592, 2592, 2592, 2592, 2592, 2592, 2592, 2592, 2592, 2592, 2592, 2592, 2592, 2592, 2592, 2592, 2592, 2592, 2592, 2592, 2592, 2592, 2592, 2592, 2592, 2592, 2592, 2048, 2048, 2592, 2592, 2592, 2592, 2592, 2592, 2592, 2592, 2592, 2592, 2592, 2592, 2592, 2592, 2592, 2592, 2592, 2592, 2592, 2592, 2592, 2592, 2592, 2592, 2592, 2592, 2592, 2592, 2592, 2592, 2592, 2592, 2592, 2592, 2592, 2036, 2020, 2004, 1988, 1972, 1956, 1940, 1924, 1908, 1892, 1876, 1860, 1844, 1828, 1812, 1796, 1780, 1764, 1748, 1732, 1716, 1700, 1684, 1668, 1652, 1636, 1620, 1604, 1588, 1572, 1556, 1540, 2592, 2592, 2592, 2592, 2592, 2592, 2592, 2592, 2592, 2592, 2592, 2592, 2592, 2592, 2592, 2592, 2592, 2592, 2592, 2592, 2592, 2592, 2592, 2592, 2592, 2592, 2592, 2592, 2592, 2592, 2592, 2592, 2040, 2024, 2008, 1992, 1976, 1960, 1944, 1928, 1912, 1896, 1880, 1864, 1848, 1832, 1816, 1800, 1784, 1768, 1752, 1736, 1720, 1704, 1688, 1672, 1656, 1640, 1624, 1608, 1592, 1576, 1560, 1544, 2592, 2592, 2592, 2592, 2592, 2592, 2592, 2592, 2592, 2592, 2592, 2592, 2592, 2592, 2592, 2592, 2592, 2592, 2592, 2592, 2592, 2592, 2592, 2592, 2592, 2592, 2592, 2592, 2592, 2592, 2592, 2592, 2044, 2028, 2012, 1996, 1980, 1964, 1948, 1932, 1916, 1900, 1884, 1868, 1852, 1836, 1820, 1804, 1788, 1772, 1756, 1740, 1724, 1708, 1692, 1676, 1660, 1644, 1628, 1612, 1596, 1580, 1564, 1548, 2592, 2592, 2592, 2592, 2592, 2592, 2592, 2592, 2592, 2592, 2592, 2592, 2592, 2592, 2592, 2592, 2592, 2592, 2592, 2592, 2592, 2592, 2592, 2592, 2592, 2592, 2592, 2592, 2592, 2592, 2592, 2592, 2048, 2032, 2016, 2000, 1984, 1968, 1952, 1936, 1920, 1904, 1888, 1872, 1856, 1840, 1824, 1808, 1792, 1776, 1760, 1744, 1728, 1712, 1696, 1680, 1664, 1648, 1632, 1616, 1600, 1584, 1568, 1552, 2592, 2592, 2592, 2592, 2592, 2592, 2592, 2592, 2592, 2592, 2592, 2592, 2592, 2592, 2592, 2592, 2556, 2548, 2540, 2532, 2524, 2516, 2508, 2500, 2492, 2484, 2476, 2468, 2460, 2452, 2444, 2436, 2428, 2420, 2412, 2404, 2396, 2388, 2380, 2372, 2364, 2356, 2348, 2340, 2332, 2324, 2316, 2308, 2300, 2292, 2284, 2276, 2268, 2260, 2252, 2244, 2236, 2228, 2220, 2212, 2204, 2196, 2188, 2180, 2172, 2164, 2156, 2148, 2140, 2132, 2124, 2116, 2108, 2100, 2092, 2084, 2076, 2068, 2060, 2052, 2592, 2592, 2592, 2592, 2592, 2592, 2592, 2592, 2592, 2592, 2592, 2592, 2592, 2592, 2592, 2592, 2560, 2552, 2544, 2536, 2528, 2520, 2512, 2504, 2496, 2488, 2480, 2472, 2464, 2456, 2448, 2440, 2432, 2424, 2416, 2408, 2400, 2392, 2384, 2376, 2368, 2360, 2352, 2344, 2336, 2328, 2320, 2312, 2304, 2296, 2288, 2280, 2272, 2264, 2256, 2248, 2240, 2232, 2224, 2216, 2208, 2200, 2192, 2184, 2176, 2168, 2160, 2152, 2144, 2136, 2128, 2120, 2112, 2104, 2096, 2088, 2080, 2072, 2064, 2056, 2592, 2592, 2592, 2592, 2592, 2592, 2592, 2592, 2592, 2592, 2592, 2592, 2592, 2592, 2592, 2592, 2592, 2592, 2592, 2592, 2592, 2592, 2592
#define FFN_ASM \
    "v_and_b32 v0, 63, %0\n\t" \
    "s_and_b32 s92, %5, 7\n\t" \
    "s_lshr_b32 s33, %5, 3\n\t" \
    "s_mov_b64 s[36:37], %3\n\t" \
    "s_and_b32 s37, s37, 0xffff\n\t" \
    "s_mov_b32 s38, 0xf00000\n\t" \
    "s_mov_b32 s39, 0x20000\n\t" \
    "s_and_b32 s34, s92, 3\n\t" \
    "s_lshl_b32 s34, s34, 2\n\t" \
    "s_and_b32 s35, s33, 3\n\t" \
    "s_add_u32 s34, s34, s35\n\t" \
    "s_lshr_b32 s35, s92, 2\n\t" \
    "s_lshl_b32 s35, s35, 3\n\t" \
    "s_lshr_b32 s49, s33, 2\n\t" \
    "s_add_u32 s35, s35, s49\n\t" \
    "s_lshl_b32 s35, s35, 2\n\t" \
    "s_and_b32 s49, %1, 3\n\t" \
    "s_add_u32 s35, s35, s49\n\t" \
    "s_lshr_b32 s58, s34, 2\n\t" \
    "s_lshl_b32 s58, s58, 1\n\t" \
    "s_and_b32 s57, s34, 1\n\t" \
    "s_bfe_u32 s59, s34, 0x10001\n\t" \
    "s_cmp_lg_u32 %6, 0\n\t" \
    "s_cbranch_scc1 .Lffn_end\n\t" \
    "s_cmp_lt_u32 %1, 4\n\t" \
    "s_cbranch_scc1 .Lcomp_%=\n\t" \
    "s_lshl_b32 s51, s35, 11\n\t" \
    "s_add_u32 s51, s51, 0x650000\n\t" \
    "s_lshr_b32 s49, s35, 2\n\t" \
    "s_lshl_b32 s49, s49, 9\n\t" \
    "s_sub_u32 s35, %1, 4\n\t" \
    "s_lshl_b32 s50, s35, 16\n\t" \
    "s_add_u32 s49, s49, s50\n\t" \
    "s_mov_b32 s50, s58\n\t" \
    "s_lshl_b32 s46, s35, 12\n\t" \
    "s_mul_i32 s44, s34, 229376\n\t" \
    "s_add_u32 s44, s44, s46\n\t" \
    "s_add_u32 s44, s44, 0x200000\n\t" \
    "v_lshlrev_b32 v1, 4, v0\n\t" \
    "v_lshrrev_b32 v8, 5, v0\n\t" \
    "v_and_b32 v32, 0x1f0, v1\n\t" \
    "v_lshl_add_u32 v32, v8, 13, v32\n\t" \
    "v_add_u32 v33, 0x3c00, v32\n\t" \
    "v_add_u32 v34, 0x7800, v32\n\t" \
    "v_add_u32 v35, 0xb400, v32\n\t" \
    "v_xor_b32 v34, 0x80, v34\n\t" \
    "v_xor_b32 v35, 0x80, v35\n\t" \
    "s_add_u32 m0, s46, 0x0\n\t" \
    "s_add_u32 s60, s50, 0\n\t" \
    "s_and_b32 s60, s60, 7\n\t" \
    "s_lshl_b32 s60, s60, 18\n\t" \
    "s_add_u32 s60, s60, s49\n\t" \
    "buffer_load_dwordx4 v32, s[36:39], s60 offen lds\n\t" \
    "buffer_load_dwordx4 v33, s[36:39], s60 offen offset:1024 lds\n\t" \
    "buffer_load_dwordx4 v34, s[36:39], s60 offen offset:2048 lds\n\t" \
    "buffer_load_dwordx4 v35, s[36:39], s60 offen offset:3072 lds\n\t" \
    "s_add_u32 m0, s46, 0x4000\n\t" \
    "s_add_u32 s60, s44, 0x0\n\t" \
    "buffer_load_dwordx4 v1, s[36:39], s60 offen lds\n\t" \
    "buffer_load_dwordx4 v1, s[36:39], s60 offen offset:1024 lds\n\t" \
    "buffer_load_dwordx4 v1, s[36:39], s60 offen offset:2048 lds\n\t" \
    "buffer_load_dwordx4 v1, s[36:39], s60 offen offset:3072 lds\n\t" \
    "s_barrier\n\t" \
    "s_add_u32 m0, s46, 0x8000\n\t" \
    "s_add_u32 s60, s50, 1\n\t" \
    "s_and_b32 s60, s60, 7\n\t" \
    "s_lshl_b32 s60, s60, 18\n\t" \
    "s_add_u32 s60, s60, s49\n\t" \
    "buffer_load_dwordx4 v32, s[36:39], s60 offen lds\n\t" \
    "buffer_load_dwordx4 v33, s[36:39], s60 offen offset:1024 lds\n\t" \
    "buffer_load_dwordx4 v34, s[36:39], s60 offen offset:2048 lds\n\t" \
    "buffer_load_dwordx4 v35, s[36:39], s60 offen offset:3072 lds\n\t" \
    "s_add_u32 m0, s46, 0xc000\n\t" \
    "s_add_u32 s60, s44, 0x4000\n\t" \
    "buffer_load_dwordx4 v1, s[36:39], s60 offen lds\n\t" \
    "buffer_load_dwordx4 v1, s[36:39], s60 offen offset:1024 lds\n\t" \
    "buffer_load_dwordx4 v1, s[36:39], s60 offen offset:2048 lds\n\t" \
    "buffer_load_dwordx4 v1, s[36:39], s60 offen offset:3072 lds\n\t" \
    "s_mov_b64 s[40:41], %2\n\t" \
    "s_and_b32 s41, s41, 0xffff\n\t" \
    "s_mov_b32 s42, 0x1c0000\n\t" \
    "s_mov_b32 s43, 0x20000\n\t" \
    "s_lshl_b32 s62, s92, 2\n\t" \
    "s_lshr_b32 s63, s33, 3\n\t" \
    "s_add_u32 s62, s62, s63\n\t" \
    "s_lshl_b32 s62, s62, 8\n\t" \
    "s_mul_i32 s45, s35, 229376\n\t" \
    "s_and_b32 s33, s33, 7\n\t" \
    "s_lshl_b32 s63, s33, 18\n\t" \
    "s_add_u32 s62, s62, s63\n\t" \
    "s_lshl_b32 s63, s35, 16\n\t" \
    "s_add_u32 s62, s62, s63\n\t" \
    "s_lshl_b32 s33, s33, 7\n\t" \
    "s_add_u32 s45, s45, s33\n\t" \
    "v_add_u32 v40, s46, v1\n\t" \
    "v_lshrrev_b32 v28, 4, v0\n\t" \
    "v_and_b32 v29, 0xf0, v1\n\t" \
    "v_lshl_add_u32 v28, v28, 13, v29\n\t" \
    "v_lshrrev_b32 v29, 3, v0\n\t" \
    "v_lshlrev_b32 v29, 10, v29\n\t" \
    "v_bfe_u32 v30, v0, 4, 1\n\t" \
    "v_lshlrev_b32 v30, 2, v30\n\t" \
    "v_and_b32 v31, 7, v0\n\t" \
    "v_xor_b32 v30, v30, v31\n\t" \
    "v_lshl_add_u32 v2, v30, 4, v29\n\t" \
    "v_add_u32 v3, 0x1c00, v2\n\t" \
    "v_add_u32 v4, 0x1b800, v2\n\t" \
    "v_add_u32 v5, 0x1d400, v2\n\t" \
    "s_waitcnt vmcnt(12)\n\t" \
    "s_waitcnt vmcnt(8)\n\t" \
    "s_barrier\n\t" \
    "s_mul_i32 s48, s35, 224\n\t" \
    "s_mul_i32 s47, s34, 896\n\t" \
    "s_add_u32 s47, s47, s48\n\t" \
    "s_add_u32 s47, s47, 0x600000\n\t" \
    "v_lshrrev_b32 v8, 4, v0\n\t" \
    "v_mul_u32_u24 v8, 0x3800, v8\n\t" \
    "v_and_b32 v9, 15, v0\n\t" \
    "v_min_u32 v9, 13, v9\n\t" \
    "v_lshl_add_u32 v8, v9, 4, v8\n\t" \
    "s_lshr_b32 s63, s46, 1\n\t" \
    "s_add_u32 m0, s63, 0x24400\n\t" \
    "s_add_u32 s60, s47, 0x0\n\t" \
    "buffer_load_dwordx4 v8, s[36:39], s60 offen lds\n\t" \
    "s_lshr_b32 s63, s46, 1\n\t" \
    "s_add_u32 m0, s63, 0x24800\n\t" \
    "s_add_u32 s60, s47, 0xe000\n\t" \
    "buffer_load_dwordx4 v8, s[36:39], s60 offen lds\n\t" \
    "s_add_u32 m0, s46, 0x10000\n\t" \
    "s_add_u32 s60, s50, 2\n\t" \
    "s_and_b32 s60, s60, 7\n\t" \
    "s_lshl_b32 s60, s60, 18\n\t" \
    "s_add_u32 s60, s60, s49\n\t" \
    "buffer_load_dwordx4 v32, s[36:39], s60 offen lds\n\t" \
    "buffer_load_dwordx4 v33, s[36:39], s60 offen offset:1024 lds\n\t" \
    "buffer_load_dwordx4 v34, s[36:39], s60 offen offset:2048 lds\n\t" \
    "buffer_load_dwordx4 v35, s[36:39], s60 offen offset:3072 lds\n\t" \
    "s_add_u32 m0, s46, 0x14000\n\t" \
    "s_add_u32 s60, s44, 0x8000\n\t" \
    "buffer_load_dwordx4 v1, s[36:39], s60 offen lds\n\t" \
    "buffer_load_dwordx4 v1, s[36:39], s60 offen offset:1024 lds\n\t" \
    "buffer_load_dwordx4 v1, s[36:39], s60 offen offset:2048 lds\n\t" \
    "buffer_load_dwordx4 v1, s[36:39], s60 offen offset:3072 lds\n\t" \
    "s_waitcnt vmcnt(14)\n\t" \
    "s_waitcnt vmcnt(10)\n\t" \
    "s_add_u32 m0, s46, 0x18000\n\t" \
    "s_add_u32 s60, s50, 3\n\t" \
    "s_and_b32 s60, s60, 7\n\t" \
    "s_lshl_b32 s60, s60, 18\n\t" \
    "s_add_u32 s60, s60, s49\n\t" \
    "s_barrier\n\t" \
    "buffer_load_dwordx4 v32, s[36:39], s60 offen lds\n\t" \
    "buffer_load_dwordx4 v33, s[36:39], s60 offen offset:1024 lds\n\t" \
    "buffer_load_dwordx4 v34, s[36:39], s60 offen offset:2048 lds\n\t" \
    "buffer_load_dwordx4 v35, s[36:39], s60 offen offset:3072 lds\n\t" \
    "s_add_u32 m0, s46, 0x1c000\n\t" \
    "s_add_u32 s60, s50, 4\n\t" \
    "s_and_b32 s60, s60, 7\n\t" \
    "s_lshl_b32 s60, s60, 18\n\t" \
    "s_add_u32 s60, s60, s49\n\t" \
    "buffer_load_dwordx4 v32, s[36:39], s60 offen lds\n\t" \
    "buffer_load_dwordx4 v33, s[36:39], s60 offen offset:1024 lds\n\t" \
    "buffer_load_dwordx4 v34, s[36:39], s60 offen offset:2048 lds\n\t" \
    "buffer_load_dwordx4 v35, s[36:39], s60 offen offset:3072 lds\n\t" \
    "s_waitcnt vmcnt(12)\n\t" \
    "s_waitcnt vmcnt(8)\n\t" \
    "s_add_u32 m0, s46, 0x20000\n\t" \
    "s_add_u32 s60, s44, 0xc000\n\t" \
    "s_barrier\n\t" \
    "buffer_load_dwordx4 v1, s[36:39], s60 offen lds\n\t" \
    "buffer_load_dwordx4 v1, s[36:39], s60 offen offset:1024 lds\n\t" \
    "buffer_load_dwordx4 v1, s[36:39], s60 offen offset:2048 lds\n\t" \
    "buffer_load_dwordx4 v1, s[36:39], s60 offen offset:3072 lds\n\t" \
    "s_add_u32 m0, s46, 0x0\n\t" \
    "s_add_u32 s60, s50, 5\n\t" \
    "s_and_b32 s60, s60, 7\n\t" \
    "s_lshl_b32 s60, s60, 18\n\t" \
    "s_add_u32 s60, s60, s49\n\t" \
    "buffer_load_dwordx4 v32, s[36:39], s60 offen lds\n\t" \
    "buffer_load_dwordx4 v33, s[36:39], s60 offen offset:1024 lds\n\t" \
    "buffer_load_dwordx4 v34, s[36:39], s60 offen offset:2048 lds\n\t" \
    "buffer_load_dwordx4 v35, s[36:39], s60 offen offset:3072 lds\n\t" \
    "s_waitcnt vmcnt(12)\n\t" \
    "s_add_u32 m0, s46, 0x4000\n\t" \
    "s_add_u32 s60, s44, 0x10000\n\t" \
    "s_barrier\n\t" \
    "buffer_load_dwordx4 v1, s[36:39], s60 offen lds\n\t" \
    "buffer_load_dwordx4 v1, s[36:39], s60 offen offset:1024 lds\n\t" \
    "buffer_load_dwordx4 v1, s[36:39], s60 offen offset:2048 lds\n\t" \
    "buffer_load_dwordx4 v1, s[36:39], s60 offen offset:3072 lds\n\t" \
    "s_add_u32 m0, s46, 0x8000\n\t" \
    "s_add_u32 s60, s50, 6\n\t" \
    "s_and_b32 s60, s60, 7\n\t" \
    "s_lshl_b32 s60, s60, 18\n\t" \
    "s_add_u32 s60, s60, s49\n\t" \
    "buffer_load_dwordx4 v32, s[36:39], s60 offen lds\n\t" \
    "buffer_load_dwordx4 v33, s[36:39], s60 offen offset:1024 lds\n\t" \
    "buffer_load_dwordx4 v34, s[36:39], s60 offen offset:2048 lds\n\t" \
    "buffer_load_dwordx4 v35, s[36:39], s60 offen offset:3072 lds\n\t" \
    "s_waitcnt vmcnt(16)\n\t" \
    "s_waitcnt vmcnt(12)\n\t" \
    "s_add_u32 m0, s46, 0xc000\n\t" \
    "s_add_u32 s60, s44, 0x14000\n\t" \
    "s_barrier\n\t" \
    "buffer_load_dwordx4 v1, s[36:39], s60 offen lds\n\t" \
    "buffer_load_dwordx4 v1, s[36:39], s60 offen offset:1024 lds\n\t" \
    "buffer_load_dwordx4 v1, s[36:39], s60 offen offset:2048 lds\n\t" \
    "buffer_load_dwordx4 v1, s[36:39], s60 offen offset:3072 lds\n\t" \
    "s_add_u32 m0, s46, 0x10000\n\t" \
    "s_add_u32 s60, s50, 7\n\t" \
    "s_and_b32 s60, s60, 7\n\t" \
    "s_lshl_b32 s60, s60, 18\n\t" \
    "s_add_u32 s60, s60, s49\n\t" \
    "buffer_load_dwordx4 v32, s[36:39], s60 offen lds\n\t" \
    "buffer_load_dwordx4 v33, s[36:39], s60 offen offset:1024 lds\n\t" \
    "buffer_load_dwordx4 v34, s[36:39], s60 offen offset:2048 lds\n\t" \
    "buffer_load_dwordx4 v35, s[36:39], s60 offen offset:3072 lds\n\t" \
    "s_waitcnt vmcnt(16)\n\t" \
    "s_waitcnt vmcnt(12)\n\t" \
    "s_barrier\n\t" \
    "s_lshr_b32 s63, s46, 1\n\t" \
    "s_add_u32 m0, s63, 0x24400\n\t" \
    "s_nop 0\n\t" \
    "buffer_load_dwordx4 v1, s[36:39], s51 offen sc1 lds\n\t" \
    "buffer_load_dwordx4 v1, s[36:39], s51 offen offset:1024 sc1 lds\n\t" \
    "s_add_u32 m0, s46, 0x18000\n\t" \
    "s_add_u32 s60, s44, 0x18000\n\t" \
    "buffer_load_dwordx4 v1, s[36:39], s60 offen lds\n\t" \
    "buffer_load_dwordx4 v1, s[36:39], s60 offen offset:1024 lds\n\t" \
    "buffer_load_dwordx4 v1, s[36:39], s60 offen offset:2048 lds\n\t" \
    "buffer_load_dwordx4 v1, s[36:39], s60 offen offset:3072 lds\n\t" \
    "s_waitcnt vmcnt(14)\n\t" \
    "s_waitcnt vmcnt(10)\n\t" \
    "s_add_u32 m0, s46, 0x14000\n\t" \
    "s_add_u32 s60, s44, 0x1c000\n\t" \
    "s_barrier\n\t" \
    "buffer_load_dwordx4 v1, s[36:39], s60 offen lds\n\t" \
    "buffer_load_dwordx4 v1, s[36:39], s60 offen offset:1024 lds\n\t" \
    "buffer_load_dwordx4 v1, s[36:39], s60 offen offset:2048 lds\n\t" \
    "buffer_load_dwordx4 v1, s[36:39], s60 offen offset:3072 lds\n\t" \
    "s_add_u32 m0, s46, 0x1c000\n\t" \
    "s_add_u32 s60, s44, 0x20000\n\t" \
    "buffer_load_dwordx4 v1, s[36:39], s60 offen lds\n\t" \
    "buffer_load_dwordx4 v1, s[36:39], s60 offen offset:1024 lds\n\t" \
    "buffer_load_dwordx4 v1, s[36:39], s60 offen offset:2048 lds\n\t" \
    "buffer_load_dwordx4 v1, s[36:39], s60 offen offset:3072 lds\n\t" \
    "s_waitcnt vmcnt(14)\n\t" \
    "s_waitcnt vmcnt(12)\n\t" \
    "s_add_u32 m0, s46, 0x20000\n\t" \
    "s_add_u32 s60, s44, 0x24000\n\t" \
    "s_barrier\n\t" \
    "buffer_load_dwordx4 v1, s[36:39], s60 offen lds\n\t" \
    "buffer_load_dwordx4 v1, s[36:39], s60 offen offset:1024 lds\n\t" \
    "buffer_load_dwordx4 v1, s[36:39], s60 offen offset:2048 lds\n\t" \
    "buffer_load_dwordx4 v1, s[36:39], s60 offen offset:3072 lds\n\t" \
    "s_waitcnt vmcnt(12)\n\t" \
    "s_add_u32 m0, s46, 0x0\n\t" \
    "s_add_u32 s60, s44, 0x28000\n\t" \
    "s_barrier\n\t" \
    "buffer_load_dwordx4 v1, s[36:39], s60 offen lds\n\t" \
    "buffer_load_dwordx4 v1, s[36:39], s60 offen offset:1024 lds\n\t" \
    "buffer_load_dwordx4 v1, s[36:39], s60 offen offset:2048 lds\n\t" \
    "buffer_load_dwordx4 v1, s[36:39], s60 offen offset:3072 lds\n\t" \
    "s_add_u32 m0, s46, 0x4000\n\t" \
    "s_add_u32 s60, s44, 0x2c000\n\t" \
    "buffer_load_dwordx4 v1, s[36:39], s60 offen lds\n\t" \
    "buffer_load_dwordx4 v1, s[36:39], s60 offen offset:1024 lds\n\t" \
    "buffer_load_dwordx4 v1, s[36:39], s60 offen offset:2048 lds\n\t" \
    "buffer_load_dwordx4 v1, s[36:39], s60 offen offset:3072 lds\n\t" \
    "s_waitcnt vmcnt(16)\n\t" \
    "s_add_u32 m0, s46, 0x8000\n\t" \
    "s_add_u32 s60, s44, 0x30000\n\t" \
    "s_barrier\n\t" \
    "buffer_load_dwordx4 v1, s[36:39], s60 offen lds\n\t" \
    "buffer_load_dwordx4 v1, s[36:39], s60 offen offset:1024 lds\n\t" \
    "buffer_load_dwordx4 v1, s[36:39], s60 offen offset:2048 lds\n\t" \
    "buffer_load_dwordx4 v1, s[36:39], s60 offen offset:3072 lds\n\t" \
    "s_add_u32 m0, s46, 0xc000\n\t" \
    "s_add_u32 s60, s44, 0x34000\n\t" \
    "buffer_load_dwordx4 v1, s[36:39], s60 offen lds\n\t" \
    "buffer_load_dwordx4 v1, s[36:39], s60 offen offset:1024 lds\n\t" \
    "buffer_load_dwordx4 v1, s[36:39], s60 offen offset:2048 lds\n\t" \
    "buffer_load_dwordx4 v1, s[36:39], s60 offen offset:3072 lds\n\t" \
    "s_waitcnt vmcnt(20)\n\t" \
    "s_barrier\n\t" \
    "s_lshr_b32 s63, s46, 1\n\t" \
    "s_add_u32 m0, s63, 0x24400\n\t" \
    "s_add_u32 s60, s62, 0x0\n\t" \
    "buffer_load_dwordx4 v28, s[36:39], s60 offen lds\n\t" \
    "s_lshr_b32 s63, s46, 1\n\t" \
    "s_add_u32 m0, s63, 0x24800\n\t" \
    "s_add_u32 s60, s62, 0x8000\n\t" \
    "buffer_load_dwordx4 v28, s[36:39], s60 offen lds\n\t" \
    "s_add_u32 m0, s46, 0x10000\n\t" \
    "s_add_u32 s60, s45, 0x0\n\t" \
    "buffer_load_dwordx4 v2, s[40:43], s60 offen lds\n\t" \
    "buffer_load_dwordx4 v3, s[40:43], s60 offen offset:1024 lds\n\t" \
    "buffer_load_dwordx4 v4, s[40:43], s60 offen offset:2048 lds\n\t" \
    "buffer_load_dwordx4 v5, s[40:43], s60 offen offset:3072 lds\n\t" \
    "s_add_u32 m0, s46, 0x18000\n\t" \
    "s_add_u32 s60, s45, 0xe0000\n\t" \
    "buffer_load_dwordx4 v2, s[40:43], s60 offen lds\n\t" \
    "buffer_load_dwordx4 v3, s[40:43], s60 offen offset:1024 lds\n\t" \
    "buffer_load_dwordx4 v4, s[40:43], s60 offen offset:2048 lds\n\t" \
    "buffer_load_dwordx4 v5, s[40:43], s60 offen offset:3072 lds\n\t" \
    "s_waitcnt vmcnt(26)\n\t" \
    "s_add_u32 m0, s46, 0x14000\n\t" \
    "s_add_u32 s60, s45, 0x4000\n\t" \
    "s_barrier\n\t" \
    "buffer_load_dwordx4 v2, s[40:43], s60 offen lds\n\t" \
    "buffer_load_dwordx4 v3, s[40:43], s60 offen offset:1024 lds\n\t" \
    "buffer_load_dwordx4 v4, s[40:43], s60 offen offset:2048 lds\n\t" \
    "buffer_load_dwordx4 v5, s[40:43], s60 offen offset:3072 lds\n\t" \
    "s_waitcnt vmcnt(26)\n\t" \
    "s_add_u32 m0, s46, 0x1c000\n\t" \
    "s_add_u32 s60, s45, 0xe4000\n\t" \
    "s_barrier\n\t" \
    "buffer_load_dwordx4 v2, s[40:43], s60 offen lds\n\t" \
    "buffer_load_dwordx4 v3, s[40:43], s60 offen offset:1024 lds\n\t" \
    "buffer_load_dwordx4 v4, s[40:43], s60 offen offset:2048 lds\n\t" \
    "buffer_load_dwordx4 v5, s[40:43], s60 offen offset:3072 lds\n\t" \
    "s_waitcnt vmcnt(26)\n\t" \
    "s_waitcnt vmcnt(16)\n\t" \
    "s_add_u32 m0, s46, 0x20000\n\t" \
    "s_add_u32 s60, s45, 0x8000\n\t" \
    "s_barrier\n\t" \
    "buffer_load_dwordx4 v2, s[40:43], s60 offen lds\n\t" \
    "buffer_load_dwordx4 v3, s[40:43], s60 offen offset:1024 lds\n\t" \
    "buffer_load_dwordx4 v4, s[40:43], s60 offen offset:2048 lds\n\t" \
    "buffer_load_dwordx4 v5, s[40:43], s60 offen offset:3072 lds\n\t" \
    "s_add_u32 m0, s46, 0x0\n\t" \
    "s_add_u32 s60, s45, 0xe8000\n\t" \
    "s_barrier\n\t" \
    "buffer_load_dwordx4 v2, s[40:43], s60 offen lds\n\t" \
    "buffer_load_dwordx4 v3, s[40:43], s60 offen offset:1024 lds\n\t" \
    "buffer_load_dwordx4 v4, s[40:43], s60 offen offset:2048 lds\n\t" \
    "buffer_load_dwordx4 v5, s[40:43], s60 offen offset:3072 lds\n\t" \
    "s_add_u32 m0, s46, 0x4000\n\t" \
    "s_add_u32 s60, s45, 0xc000\n\t" \
    "s_barrier\n\t" \
    "buffer_load_dwordx4 v2, s[40:43], s60 offen lds\n\t" \
    "buffer_load_dwordx4 v3, s[40:43], s60 offen offset:1024 lds\n\t" \
    "buffer_load_dwordx4 v4, s[40:43], s60 offen offset:2048 lds\n\t" \
    "buffer_load_dwordx4 v5, s[40:43], s60 offen offset:3072 lds\n\t" \
    "s_waitcnt vmcnt(24)\n\t" \
    "s_add_u32 m0, s46, 0x8000\n\t" \
    "s_add_u32 s60, s45, 0xec000\n\t" \
    "s_barrier\n\t" \
    "buffer_load_dwordx4 v2, s[40:43], s60 offen lds\n\t" \
    "buffer_load_dwordx4 v3, s[40:43], s60 offen offset:1024 lds\n\t" \
    "buffer_load_dwordx4 v4, s[40:43], s60 offen offset:2048 lds\n\t" \
    "buffer_load_dwordx4 v5, s[40:43], s60 offen offset:3072 lds\n\t" \
    "s_waitcnt vmcnt(24)\n\t" \
    "s_add_u32 m0, s46, 0xc000\n\t" \
    "s_add_u32 s60, s45, 0x10000\n\t" \
    "s_barrier\n\t" \
    "buffer_load_dwordx4 v2, s[40:43], s60 offen lds\n\t" \
    "buffer_load_dwordx4 v3, s[40:43], s60 offen offset:1024 lds\n\t" \
    "buffer_load_dwordx4 v4, s[40:43], s60 offen offset:2048 lds\n\t" \
    "buffer_load_dwordx4 v5, s[40:43], s60 offen offset:3072 lds\n\t" \
    "s_waitcnt vmcnt(24)\n\t" \
    "s_add_u32 m0, s46, 0x10000\n\t" \
    "s_add_u32 s60, s45, 0xf0000\n\t" \
    "s_barrier\n\t" \
    "buffer_load_dwordx4 v2, s[40:43], s60 offen lds\n\t" \
    "buffer_load_dwordx4 v3, s[40:43], s60 offen offset:1024 lds\n\t" \
    "buffer_load_dwordx4 v4, s[40:43], s60 offen offset:2048 lds\n\t" \
    "buffer_load_dwordx4 v5, s[40:43], s60 offen offset:3072 lds\n\t" \
    "s_waitcnt vmcnt(24)\n\t" \
    "s_add_u32 m0, s46, 0x18000\n\t" \
    "s_add_u32 s60, s45, 0x14000\n\t" \
    "s_barrier\n\t" \
    "buffer_load_dwordx4 v2, s[40:43], s60 offen lds\n\t" \
    "buffer_load_dwordx4 v3, s[40:43], s60 offen offset:1024 lds\n\t" \
    "buffer_load_dwordx4 v4, s[40:43], s60 offen offset:2048 lds\n\t" \
    "buffer_load_dwordx4 v5, s[40:43], s60 offen offset:3072 lds\n\t" \
    "s_waitcnt vmcnt(24)\n\t" \
    "s_add_u32 m0, s46, 0x14000\n\t" \
    "s_add_u32 s60, s45, 0xf4000\n\t" \
    "s_barrier\n\t" \
    "buffer_load_dwordx4 v2, s[40:43], s60 offen lds\n\t" \
    "buffer_load_dwordx4 v3, s[40:43], s60 offen offset:1024 lds\n\t" \
    "buffer_load_dwordx4 v4, s[40:43], s60 offen offset:2048 lds\n\t" \
    "buffer_load_dwordx4 v5, s[40:43], s60 offen offset:3072 lds\n\t" \
    "s_waitcnt vmcnt(24)\n\t" \
    "s_add_u32 m0, s46, 0x1c000\n\t" \
    "s_add_u32 s60, s45, 0x18000\n\t" \
    "s_barrier\n\t" \
    "buffer_load_dwordx4 v2, s[40:43], s60 offen lds\n\t" \
    "buffer_load_dwordx4 v3, s[40:43], s60 offen offset:1024 lds\n\t" \
    "buffer_load_dwordx4 v4, s[40:43], s60 offen offset:2048 lds\n\t" \
    "buffer_load_dwordx4 v5, s[40:43], s60 offen offset:3072 lds\n\t" \
    "s_waitcnt vmcnt(24)\n\t" \
    "s_add_u32 m0, s46, 0x20000\n\t" \
    "s_add_u32 s60, s45, 0xf8000\n\t" \
    "s_barrier\n\t" \
    "buffer_load_dwordx4 v2, s[40:43], s60 offen lds\n\t" \
    "buffer_load_dwordx4 v3, s[40:43], s60 offen offset:1024 lds\n\t" \
    "buffer_load_dwordx4 v4, s[40:43], s60 offen offset:2048 lds\n\t" \
    "buffer_load_dwordx4 v5, s[40:43], s60 offen offset:3072 lds\n\t" \
    "s_waitcnt vmcnt(24)\n\t" \
    "s_barrier\n\t" \
    "s_waitcnt vmcnt(20)\n\t" \
    "s_barrier\n\t" \
    "s_waitcnt vmcnt(16)\n\t" \
    "s_barrier\n\t" \
    "s_waitcnt vmcnt(12)\n\t" \
    "s_barrier\n\t" \
    "s_waitcnt vmcnt(8)\n\t" \
    "s_barrier\n\t" \
    "s_waitcnt vmcnt(4)\n\t" \
    "s_barrier\n\t" \
    "s_waitcnt vmcnt(0)\n\t" \
    "s_barrier\n\t" \
    "s_endpgm\n\t" \
    ".Lcomp_%=:\n\t" \
    "s_barrier\n\t" \
    "v_lshrrev_b32 v13, 4, v0\n\t" \
    "v_and_b32 v14, 15, v0\n\t" \
    "s_add_u32 s50, s58, s57\n\t" \
    "s_lshl_b32 s50, s50, 8\n\t" \
    "s_lshl_b32 s51, s59, 3\n\t" \
    "s_add_u32 s60, s50, s51\n\t" \
    "s_lshl_b32 s57, s57, 1\n\t" \
    "s_add_u32 s63, s57, s59\n\t" \
    "s_lshl_b32 s44, s35, 11\n\t" \
    "s_add_u32 s44, s44, 0x650000\n\t" \
    "s_mov_b64 s[40:41], %4\n\t" \
    "s_and_b32 s41, s41, 0xffff\n\t" \
    "s_mov_b32 s42, 0x200000\n\t" \
    "s_mov_b32 s43, 0x20000\n\t" \
    "s_mul_i32 s45, s35, 114688\n\t" \
    "s_lshl_b32 s49, s34, 10\n\t" \
    "s_add_u32 s45, s45, s49\n\t" \
    "s_add_u32 s45, s45, 0x800000\n\t" \
    "s_lshl_b32 s35, s92, 2\n\t" \
    "s_lshr_b32 s49, s33, 3\n\t" \
    "s_add_u32 s35, s35, s49\n\t" \
    "s_lshl_b32 s35, s35, 1\n\t" \
    "s_lshr_b32 s49, %1, 1\n\t" \
    "s_add_u32 s35, s35, s49\n\t" \
    "s_and_b32 s49, %1, 1\n\t" \
    "s_and_b32 s33, s33, 7\n\t" \
    "s_mul_i32 s46, s35, 114688\n\t" \
    "s_lshl_b32 s50, s49, 10\n\t" \
    "s_add_u32 s46, s46, s50\n\t" \
    "s_add_u32 s46, s46, 0x800000\n\t" \
    "s_lshl_b32 s47, s49, 12\n\t" \
    "s_lshr_b32 s50, %1, 1\n\t" \
    "s_lshl_b32 s50, s50, 7\n\t" \
    "s_add_u32 s47, s47, s50\n\t" \
    "s_add_u32 s47, s47, 0x24400\n\t" \
    "s_lshl_b32 s48, s35, 15\n\t" \
    "s_lshl_b32 s50, s33, 7\n\t" \
    "s_add_u32 s48, s48, s50\n\t" \
    "s_lshl_b32 s50, s49, 6\n\t" \
    "s_add_u32 s48, s48, s50\n\t" \
    "v_lshlrev_b32 v1, 4, v0\n\t" \
    "v_mov_b32 v2, v1\n\t" \
    "v_and_b32 v15, 1, v13\n\t" \
    "v_xor_b32 v15, %1, v15\n\t" \
    "v_lshlrev_b32 v15, 7, v15\n\t" \
    "v_lshl_add_u32 v15, v14, 3, v15\n\t" \
    "v_lshl_add_u32 v3, v13, 11, v15\n\t" \
    "v_lshlrev_b32 v16, 4, v13\n\t" \
    "v_add_u32 v16, 0, v16\n\t" \
    "v_add_u32 v16, 0x24000, v16\n\t" \
    "v_mov_b32 v8, 0x3f803f80\n\t" \
    "v_mov_b32 v9, 0x3f803f80\n\t" \
    "v_mov_b32 v10, 0x3f803f80\n\t" \
    "v_mov_b32 v11, 0x3f803f80\n\t" \
    "s_mov_b32 s52, 0x100001\n\t" \
    "s_mov_b32 s53, 0x10000100\n\t" \
    "s_mov_b32 s54, 0x200002\n\t" \
    "s_mov_b32 s55, 0x20000200\n\t" \
    "s_mov_b32 s56, 0x400004\n\t" \
    "s_mov_b32 s57, 0x40000400\n\t" \
    "s_mov_b32 s58, 0x800008\n\t" \
    "s_mov_b32 s59, 0x80000800\n\t" \
    "v_lshrrev_b32 v12, 2, v14\n\t" \
    "v_lshrrev_b32 v15, 5, v0\n\t" \
    "v_lshl_add_u32 v15, v15, 3, v12\n\t" \
    "v_lshlrev_b32 v15, 7, v15\n\t" \
    "v_and_b32 v14, 3, v0\n\t" \
    "v_lshl_add_u32 v15, v14, 3, v15\n\t" \
    "v_and_b32 v14, 1, v13\n\t" \
    "v_lshl_add_u32 v15, v14, 5, v15\n\t" \
    "s_and_b32 s61, %1, 1\n\t" \
    "s_lshl_b32 s62, s61, 11\n\t" \
    "s_add_u32 s62, s62, 0\n\t" \
    "s_add_u32 s62, s62, 0x0\n\t" \
    "v_add_u32 v15, s62, v15\n\t" \
    "v_lshrrev_b32 v12, 1, v12\n\t" \
    "v_xor_b32 v12, s61, v12\n\t" \
    "v_lshl_add_u32 v20, v12, 6, v15\n\t" \
    "v_xor_b32 v12, 1, v12\n\t" \
    "v_lshl_add_u32 v21, v12, 6, v15\n\t" \
    "s_barrier\n\t" \
    "ds_read_b64 v[128:129], v3 offset:0\n\t" \
    "ds_read_b64 v[132:133], v3 offset:512\n\t" \
    "ds_read_b64 v[130:131], v3 offset:1024\n\t" \
    "ds_read_b64 v[134:135], v3 offset:1536\n\t" \
    "ds_read_b128 v[24:27], v2 offset:16384\n\t" \
    "ds_read_b128 v[28:31], v2 offset:17408\n\t" \
    "ds_read_b128 v[32:35], v2 offset:18432\n\t" \
    "ds_read_b128 v[36:39], v2 offset:19456\n\t" \
    "s_waitcnt lgkmcnt(4)\n\t" \
    "v_swap_b32 v129, v132\n\t" \
    "v_swap_b32 v131, v134\n\t" \
    "s_waitcnt lgkmcnt(2)\n\t" \
    "s_nop 0\n\t" \
    "v_mfma_f32_16x16x32_bf16 v[64:67], v[24:27], v[128:131], 0\n\t" \
    "ds_read_b128 v[40:43], v2 offset:20480\n\t" \
    "ds_read_b64 v[136:137], v3 offset:8192\n\t" \
    "v_mfma_f32_16x16x32_bf16 v[68:71], v[24:27], v[132:135], 0\n\t" \
    "ds_read_b64 v[140:141], v3 offset:8704\n\t" \
    "ds_read_b64 v[138:139], v3 offset:9216\n\t" \
    "v_mfma_f32_16x16x32_bf16 v[76:79], v[28:31], v[132:135], 0\n\t" \
    "ds_read_b128 v[44:47], v2 offset:21504\n\t" \
    "ds_read_b64 v[142:143], v3 offset:9728\n\t" \
    "v_mfma_f32_16x16x32_bf16 v[72:75], v[28:31], v[128:131], 0\n\t" \
    "s_waitcnt lgkmcnt(6)\n\t" \
    "v_mfma_f32_16x16x32_bf16 v[80:83], v[32:35], v[128:131], 0\n\t" \
    "ds_read_b128 v[24:27], v2 offset:22528\n\t" \
    "v_mfma_f32_16x16x32_bf16 v[84:87], v[32:35], v[132:135], 0\n\t" \
    "v_mfma_f32_16x16x32_bf16 v[92:95], v[36:39], v[132:135], 0\n\t" \
    "ds_read_b128 v[28:31], v2 offset:23552\n\t" \
    "v_mfma_f32_16x16x32_bf16 v[88:91], v[36:39], v[128:131], 0\n\t" \
    "s_waitcnt lgkmcnt(3)\n\t" \
    "v_mfma_f32_16x16x32_bf16 v[96:99], v[40:43], v[128:131], 0\n\t" \
    "ds_read_b128 v[32:35], v2 offset:24576\n\t" \
    "s_waitcnt lgkmcnt(3)\n\t" \
    "v_mfma_f32_16x16x32_bf16 v[100:103], v[40:43], v[132:135], 0\n\t" \
    "v_swap_b32 v137, v140\n\t" \
    "v_swap_b32 v139, v142\n\t" \
    "v_mfma_f32_16x16x32_bf16 v[108:111], v[44:47], v[132:135], 0\n\t" \
    "ds_read_b128 v[36:39], v2 offset:25600\n\t" \
    "v_mfma_f32_16x16x32_bf16 v[104:107], v[44:47], v[128:131], 0\n\t" \
    "s_waitcnt lgkmcnt(2)\n\t" \
    "v_mfma_f32_16x16x32_bf16 v[64:67], v[24:27], v[136:139], v[64:67]\n\t" \
    "ds_read_b128 v[40:43], v2 offset:26624\n\t" \
    "s_barrier\n\t" \
    "ds_read_b64 v[144:145], v3 offset:32768\n\t" \
    "v_mfma_f32_16x16x32_bf16 v[68:71], v[24:27], v[140:143], v[68:71]\n\t" \
    "ds_read_b64 v[148:149], v3 offset:33280\n\t" \
    "ds_read_b64 v[146:147], v3 offset:33792\n\t" \
    "v_mfma_f32_16x16x32_bf16 v[76:79], v[28:31], v[140:143], v[76:79]\n\t" \
    "ds_read_b128 v[44:47], v2 offset:27648\n\t" \
    "ds_read_b64 v[150:151], v3 offset:34304\n\t" \
    "v_mfma_f32_16x16x32_bf16 v[72:75], v[28:31], v[136:139], v[72:75]\n\t" \
    "s_waitcnt lgkmcnt(6)\n\t" \
    "v_mfma_f32_16x16x32_bf16 v[80:83], v[32:35], v[136:139], v[80:83]\n\t" \
    "ds_read_b128 v[24:27], v2 offset:28672\n\t" \
    "v_mfma_f32_16x16x32_bf16 v[84:87], v[32:35], v[140:143], v[84:87]\n\t" \
    "v_mfma_f32_16x16x32_bf16 v[92:95], v[36:39], v[140:143], v[92:95]\n\t" \
    "ds_read_b128 v[28:31], v2 offset:29696\n\t" \
    "v_mfma_f32_16x16x32_bf16 v[88:91], v[36:39], v[136:139], v[88:91]\n\t" \
    "s_waitcnt lgkmcnt(3)\n\t" \
    "v_mfma_f32_16x16x32_bf16 v[96:99], v[40:43], v[136:139], v[96:99]\n\t" \
    "ds_read_b128 v[32:35], v2 offset:30720\n\t" \
    "s_waitcnt lgkmcnt(3)\n\t" \
    "v_mfma_f32_16x16x32_bf16 v[100:103], v[40:43], v[140:143], v[100:103]\n\t" \
    "v_swap_b32 v145, v148\n\t" \
    "v_swap_b32 v147, v150\n\t" \
    "v_mfma_f32_16x16x32_bf16 v[108:111], v[44:47], v[140:143], v[108:111]\n\t" \
    "ds_read_b128 v[36:39], v2 offset:31744\n\t" \
    "v_mfma_f32_16x16x32_bf16 v[104:107], v[44:47], v[136:139], v[104:107]\n\t" \
    "s_waitcnt lgkmcnt(2)\n\t" \
    "v_mfma_f32_16x16x32_bf16 v[64:67], v[24:27], v[144:147], v[64:67]\n\t" \
    "ds_read_b128 v[40:43], v2 offset:49152\n\t" \
    "ds_read_b64 v[152:153], v3 offset:40960\n\t" \
    "v_mfma_f32_16x16x32_bf16 v[68:71], v[24:27], v[148:151], v[68:71]\n\t" \
    "ds_read_b64 v[156:157], v3 offset:41472\n\t" \
    "ds_read_b64 v[154:155], v3 offset:41984\n\t" \
    "v_mfma_f32_16x16x32_bf16 v[76:79], v[28:31], v[148:151], v[76:79]\n\t" \
    "ds_read_b128 v[44:47], v2 offset:50176\n\t" \
    "ds_read_b64 v[158:159], v3 offset:42496\n\t" \
    "v_mfma_f32_16x16x32_bf16 v[72:75], v[28:31], v[144:147], v[72:75]\n\t" \
    "s_waitcnt lgkmcnt(6)\n\t" \
    "v_mfma_f32_16x16x32_bf16 v[80:83], v[32:35], v[144:147], v[80:83]\n\t" \
    "ds_read_b128 v[24:27], v2 offset:51200\n\t" \
    "v_mfma_f32_16x16x32_bf16 v[84:87], v[32:35], v[148:151], v[84:87]\n\t" \
    "v_mfma_f32_16x16x32_bf16 v[92:95], v[36:39], v[148:151], v[92:95]\n\t" \
    "ds_read_b128 v[28:31], v2 offset:52224\n\t" \
    "v_mfma_f32_16x16x32_bf16 v[88:91], v[36:39], v[144:147], v[88:91]\n\t" \
    "s_waitcnt lgkmcnt(3)\n\t" \
    "v_mfma_f32_16x16x32_bf16 v[96:99], v[40:43], v[144:147], v[96:99]\n\t" \
    "ds_read_b128 v[32:35], v2 offset:53248\n\t" \
    "s_waitcnt lgkmcnt(3)\n\t" \
    "v_mfma_f32_16x16x32_bf16 v[100:103], v[40:43], v[148:151], v[100:103]\n\t" \
    "v_swap_b32 v153, v156\n\t" \
    "v_swap_b32 v155, v158\n\t" \
    "v_mfma_f32_16x16x32_bf16 v[108:111], v[44:47], v[148:151], v[108:111]\n\t" \
    "ds_read_b128 v[36:39], v2 offset:54272\n\t" \
    "v_mfma_f32_16x16x32_bf16 v[104:107], v[44:47], v[144:147], v[104:107]\n\t" \
    "s_waitcnt lgkmcnt(2)\n\t" \
    "v_mfma_f32_16x16x32_bf16 v[64:67], v[24:27], v[152:155], v[64:67]\n\t" \
    "ds_read_b128 v[40:43], v2 offset:55296\n\t" \
    "s_barrier\n\t" \
    "v_add_u32 v3, 0x10000, v3\n\t" \
    "ds_read_b64 v[160:161], v3 offset:0\n\t" \
    "v_mfma_f32_16x16x32_bf16 v[68:71], v[24:27], v[156:159], v[68:71]\n\t" \
    "ds_read_b64 v[164:165], v3 offset:512\n\t" \
    "ds_read_b64 v[162:163], v3 offset:1024\n\t" \
    "v_mfma_f32_16x16x32_bf16 v[76:79], v[28:31], v[156:159], v[76:79]\n\t" \
    "ds_read_b128 v[44:47], v2 offset:56320\n\t" \
    "ds_read_b64 v[166:167], v3 offset:1536\n\t" \
    "v_mfma_f32_16x16x32_bf16 v[72:75], v[28:31], v[152:155], v[72:75]\n\t" \
    "s_cmp_lg_u32 s63, 0\n\t" \
    "s_cbranch_scc1 .Lps0_%=\n\t" \
    "v_mfma_f32_16x16x32_bf16 v[48:51], v[8:11], v[128:131], 0\n\t" \
    "v_mfma_f32_16x16x32_bf16 v[52:55], v[128:131], v[128:131], 0\n\t" \
    "v_mfma_f32_16x16x32_bf16 v[48:51], v[8:11], v[136:139], v[48:51]\n\t" \
    "v_mfma_f32_16x16x32_bf16 v[52:55], v[136:139], v[136:139], v[52:55]\n\t" \
    "s_branch .Lpse_%=\n\t" \
    ".Lps0_%=:\n\t" \
    "s_cmp_lg_u32 s63, 1\n\t" \
    "s_cbranch_scc1 .Lps1_%=\n\t" \
    "v_mfma_f32_16x16x32_bf16 v[48:51], v[8:11], v[132:135], 0\n\t" \
    "v_mfma_f32_16x16x32_bf16 v[52:55], v[132:135], v[132:135], 0\n\t" \
    "v_mfma_f32_16x16x32_bf16 v[48:51], v[8:11], v[140:143], v[48:51]\n\t" \
    "v_mfma_f32_16x16x32_bf16 v[52:55], v[140:143], v[140:143], v[52:55]\n\t" \
    "s_branch .Lpse_%=\n\t" \
    ".Lps1_%=:\n\t" \
    "s_cmp_lg_u32 s63, 2\n\t" \
    "s_cbranch_scc1 .Lps2_%=\n\t" \
    "v_mfma_f32_16x16x32_bf16 v[48:51], v[8:11], v[144:147], 0\n\t" \
    "v_mfma_f32_16x16x32_bf16 v[52:55], v[144:147], v[144:147], 0\n\t" \
    "v_mfma_f32_16x16x32_bf16 v[48:51], v[8:11], v[152:155], v[48:51]\n\t" \
    "v_mfma_f32_16x16x32_bf16 v[52:55], v[152:155], v[152:155], v[52:55]\n\t" \
    "s_branch .Lpse_%=\n\t" \
    ".Lps2_%=:\n\t" \
    "v_mfma_f32_16x16x32_bf16 v[48:51], v[8:11], v[148:151], 0\n\t" \
    "v_mfma_f32_16x16x32_bf16 v[52:55], v[148:151], v[148:151], 0\n\t" \
    "v_mfma_f32_16x16x32_bf16 v[48:51], v[8:11], v[156:159], v[48:51]\n\t" \
    "v_mfma_f32_16x16x32_bf16 v[52:55], v[156:159], v[156:159], v[52:55]\n\t" \
    ".Lpse_%=:\n\t" \
    "s_waitcnt lgkmcnt(6)\n\t" \
    "v_mfma_f32_16x16x32_bf16 v[80:83], v[32:35], v[152:155], v[80:83]\n\t" \
    "ds_read_b128 v[24:27], v2 offset:57344\n\t" \
    "v_and_b32 v18, 15, v0\n\t" \
    "v_mfma_f32_16x16x32_bf16 v[84:87], v[32:35], v[156:159], v[84:87]\n\t" \
    "v_lshlrev_b32 v18, 4, v18\n\t" \
    "s_add_u32 s62, s60, s44\n\t" \
    "v_mfma_f32_16x16x32_bf16 v[92:95], v[36:39], v[156:159], v[92:95]\n\t" \
    "ds_read_b128 v[28:31], v2 offset:58368\n\t" \
    "v_cndmask_b32 v49, 0, v52, s[52:53]\n\t" \
    "v_mfma_f32_16x16x32_bf16 v[88:91], v[36:39], v[152:155], v[88:91]\n\t" \
    "v_cndmask_b32 v49, v49, v53, s[54:55]\n\t" \
    "s_waitcnt lgkmcnt(3)\n\t" \
    "v_mfma_f32_16x16x32_bf16 v[96:99], v[40:43], v[152:155], v[96:99]\n\t" \
    "ds_read_b128 v[32:35], v2 offset:59392\n\t" \
    "s_waitcnt lgkmcnt(3)\n\t" \
    "v_mfma_f32_16x16x32_bf16 v[100:103], v[40:43], v[156:159], v[100:103]\n\t" \
    "v_swap_b32 v161, v164\n\t" \
    "v_swap_b32 v163, v166\n\t" \
    "v_mfma_f32_16x16x32_bf16 v[108:111], v[44:47], v[156:159], v[108:111]\n\t" \
    "ds_read_b128 v[36:39], v2 offset:60416\n\t" \
    "v_cndmask_b32 v49, v49, v54, s[56:57]\n\t" \
    "v_mfma_f32_16x16x32_bf16 v[104:107], v[44:47], v[152:155], v[104:107]\n\t" \
    "v_cndmask_b32 v49, v49, v55, s[58:59]\n\t" \
    "s_waitcnt lgkmcnt(2)\n\t" \
    "v_mfma_f32_16x16x32_bf16 v[64:67], v[24:27], v[160:163], v[64:67]\n\t" \
    "ds_read_b128 v[40:43], v2 offset:61440\n\t" \
    "ds_read_b64 v[168:169], v3 offset:8192\n\t" \
    "v_mfma_f32_16x16x32_bf16 v[68:71], v[24:27], v[164:167], v[68:71]\n\t" \
    "ds_read_b64 v[172:173], v3 offset:8704\n\t" \
    "ds_read_b64 v[170:171], v3 offset:9216\n\t" \
    "v_mfma_f32_16x16x32_bf16 v[76:79], v[28:31], v[164:167], v[76:79]\n\t" \
    "ds_read_b128 v[44:47], v2 offset:62464\n\t" \
    "ds_read_b64 v[174:175], v3 offset:9728\n\t" \
    "v_mfma_f32_16x16x32_bf16 v[72:75], v[28:31], v[160:163], v[72:75]\n\t" \
    "v_mov_b32 v12, v49\n\t" \
    "s_waitcnt lgkmcnt(6)\n\t" \
    "v_mfma_f32_16x16x32_bf16 v[80:83], v[32:35], v[160:163], v[80:83]\n\t" \
    "ds_read_b128 v[24:27], v2 offset:63488\n\t" \
    "s_nop 0\n\t" \
    "v_mfma_f32_16x16x32_bf16 v[84:87], v[32:35], v[164:167], v[84:87]\n\t" \
    "s_nop 0\n\t" \
    "v_permlane32_swap_b32 v49, v12\n\t" \
    "v_mfma_f32_16x16x32_bf16 v[92:95], v[36:39], v[164:167], v[92:95]\n\t" \
    "ds_read_b128 v[28:31], v2 offset:64512\n\t" \
    "v_add_f32 v49, v49, v12\n\t" \
    "v_mfma_f32_16x16x32_bf16 v[88:91], v[36:39], v[160:163], v[88:91]\n\t" \
    "v_mov_b32 v12, v49\n\t" \
    "s_waitcnt lgkmcnt(3)\n\t" \
    "v_mfma_f32_16x16x32_bf16 v[96:99], v[40:43], v[160:163], v[96:99]\n\t" \
    "v_add_u32 v2, 0x10000, v2\n\t" \
    "ds_read_b128 v[32:35], v2 offset:16384\n\t" \
    "s_waitcnt lgkmcnt(3)\n\t" \
    "v_mfma_f32_16x16x32_bf16 v[100:103], v[40:43], v[164:167], v[100:103]\n\t" \
    "v_swap_b32 v169, v172\n\t" \
    "v_swap_b32 v171, v174\n\t" \
    "v_mfma_f32_16x16x32_bf16 v[108:111], v[44:47], v[164:167], v[108:111]\n\t" \
    "ds_read_b128 v[36:39], v2 offset:17408\n\t" \
    "s_nop 0\n\t" \
    "v_mfma_f32_16x16x32_bf16 v[104:107], v[44:47], v[160:163], v[104:107]\n\t" \
    "s_nop 0\n\t" \
    "s_waitcnt lgkmcnt(2)\n\t" \
    "v_mfma_f32_16x16x32_bf16 v[64:67], v[24:27], v[168:171], v[64:67]\n\t" \
    "ds_read_b128 v[40:43], v2 offset:18432\n\t" \
    "s_barrier\n\t" \
    "ds_read_b64 v[176:177], v3 offset:32768\n\t" \
    "v_mfma_f32_16x16x32_bf16 v[68:71], v[24:27], v[172:175], v[68:71]\n\t" \
    "ds_read_b64 v[180:181], v3 offset:33280\n\t" \
    "ds_read_b64 v[178:179], v3 offset:33792\n\t" \
    "v_mfma_f32_16x16x32_bf16 v[76:79], v[28:31], v[172:175], v[76:79]\n\t" \
    "ds_read_b128 v[44:47], v2 offset:19456\n\t" \
    "ds_read_b64 v[182:183], v3 offset:34304\n\t" \
    "v_mfma_f32_16x16x32_bf16 v[72:75], v[28:31], v[168:171], v[72:75]\n\t" \
    "v_permlane16_swap_b32 v49, v12\n\t" \
    "s_waitcnt lgkmcnt(6)\n\t" \
    "v_mfma_f32_16x16x32_bf16 v[80:83], v[32:35], v[168:171], v[80:83]\n\t" \
    "ds_read_b128 v[24:27], v2 offset:20480\n\t" \
    "v_add_f32 v49, v49, v12\n\t" \
    "v_mfma_f32_16x16x32_bf16 v[84:87], v[32:35], v[172:175], v[84:87]\n\t" \
    "buffer_store_dwordx2 v[48:49], v18, s[36:39], s62 offen sc1\n\t" \
    "v_lshrrev_b32 v17, 4, v0\n\t" \
    "v_mfma_f32_16x16x32_bf16 v[92:95], v[36:39], v[172:175], v[92:95]\n\t" \
    "ds_read_b128 v[28:31], v2 offset:21504\n\t" \
    "v_lshl_add_u32 v18, v17, 9, v18\n\t" \
    "v_mfma_f32_16x16x32_bf16 v[88:91], v[36:39], v[168:171], v[88:91]\n\t" \
    "s_lshl_b32 s50, %1, 11\n\t" \
    "s_waitcnt lgkmcnt(3)\n\t" \
    "v_mfma_f32_16x16x32_bf16 v[96:99], v[40:43], v[168:171], v[96:99]\n\t" \
    "ds_read_b128 v[32:35], v2 offset:22528\n\t" \
    "s_waitcnt lgkmcnt(3)\n\t" \
    "v_mfma_f32_16x16x32_bf16 v[100:103], v[40:43], v[172:175], v[100:103]\n\t" \
    "v_swap_b32 v177, v180\n\t" \
    "v_swap_b32 v179, v182\n\t" \
    "v_mfma_f32_16x16x32_bf16 v[108:111], v[44:47], v[172:175], v[108:111]\n\t" \
    "ds_read_b128 v[36:39], v2 offset:23552\n\t" \
    "s_add_u32 s50, s50, 0x24400\n\t" \
    "v_mfma_f32_16x16x32_bf16 v[104:107], v[44:47], v[168:171], v[104:107]\n\t" \
    "v_add_u32 v17, s50, v1\n\t" \
    "s_waitcnt lgkmcnt(2)\n\t" \
    "v_mfma_f32_16x16x32_bf16 v[64:67], v[24:27], v[176:179], v[64:67]\n\t" \
    "ds_read_b128 v[40:43], v2 offset:24576\n\t" \
    "ds_read_b64 v[184:185], v3 offset:40960\n\t" \
    "v_mfma_f32_16x16x32_bf16 v[68:71], v[24:27], v[180:183], v[68:71]\n\t" \
    "ds_read_b64 v[188:189], v3 offset:41472\n\t" \
    "ds_read_b64 v[186:187], v3 offset:41984\n\t" \
    "v_mfma_f32_16x16x32_bf16 v[76:79], v[28:31], v[180:183], v[76:79]\n\t" \
    "ds_read_b128 v[44:47], v2 offset:25600\n\t" \
    "ds_read_b64 v[190:191], v3 offset:42496\n\t" \
    "v_mfma_f32_16x16x32_bf16 v[72:75], v[28:31], v[176:179], v[72:75]\n\t" \
    "ds_read_b128 v[56:59], v17 offset:0\n\t" \
    "s_waitcnt lgkmcnt(7)\n\t" \
    "v_mfma_f32_16x16x32_bf16 v[80:83], v[32:35], v[176:179], v[80:83]\n\t" \
    "ds_read_b128 v[24:27], v2 offset:26624\n\t" \
    "ds_read_b128 v[60:63], v17 offset:1024\n\t" \
    "v_mfma_f32_16x16x32_bf16 v[84:87], v[32:35], v[180:183], v[84:87]\n\t" \
    "s_mul_i32 s50, %1, 224\n\t" \
    "s_add_u32 s50, s50, 0x24000\n\t" \
    "v_mfma_f32_16x16x32_bf16 v[92:95], v[36:39], v[180:183], v[92:95]\n\t" \
    "ds_read_b128 v[28:31], v2 offset:27648\n\t" \
    "v_and_b32 v17, 15, v0\n\t" \
    "v_mfma_f32_16x16x32_bf16 v[88:91], v[36:39], v[176:179], v[88:91]\n\t" \
    "v_min_u32 v17, 13, v17\n\t" \
    "s_waitcnt lgkmcnt(5)\n\t" \
    "v_mfma_f32_16x16x32_bf16 v[96:99], v[40:43], v[176:179], v[96:99]\n\t" \
    "ds_read_b128 v[32:35], v2 offset:28672\n\t" \
    "s_waitcnt lgkmcnt(5)\n\t" \
    "v_mfma_f32_16x16x32_bf16 v[100:103], v[40:43], v[180:183], v[100:103]\n\t" \
    "v_swap_b32 v185, v188\n\t" \
    "v_swap_b32 v187, v190\n\t" \
    "v_mfma_f32_16x16x32_bf16 v[108:111], v[44:47], v[180:183], v[108:111]\n\t" \
    "ds_read_b128 v[36:39], v2 offset:29696\n\t" \
    "v_lshlrev_b32 v17, 4, v17\n\t" \
    "v_mfma_f32_16x16x32_bf16 v[104:107], v[44:47], v[176:179], v[104:107]\n\t" \
    "v_add_u32 v17, s50, v17\n\t" \
    "s_waitcnt lgkmcnt(2)\n\t" \
    "v_mfma_f32_16x16x32_bf16 v[64:67], v[24:27], v[184:187], v[64:67]\n\t" \
    "ds_read_b128 v[40:43], v2 offset:30720\n\t" \
    "s_barrier\n\t" \
    "ds_read_b64 v[192:193], v3 offset:49152\n\t" \
    "v_mfma_f32_16x16x32_bf16 v[68:71], v[24:27], v[188:191], v[68:71]\n\t" \
    "ds_read_b64 v[196:197], v3 offset:49664\n\t" \
    "ds_read_b64 v[194:195], v3 offset:50176\n\t" \
    "v_mfma_f32_16x16x32_bf16 v[76:79], v[28:31], v[188:191], v[76:79]\n\t" \
    "ds_read_b128 v[44:47], v2 offset:31744\n\t" \
    "ds_read_b64 v[198:199], v3 offset:50688\n\t" \
    "v_mfma_f32_16x16x32_bf16 v[72:75], v[28:31], v[184:187], v[72:75]\n\t" \
    "v_add_f32 v56, v56, v60\n\t" \
    "s_waitcnt lgkmcnt(6)\n\t" \
    "v_mfma_f32_16x16x32_bf16 v[80:83], v[32:35], v[184:187], v[80:83]\n\t" \
    "v_add_u32 v2, 0x10000, v2\n\t" \
    "ds_read_b128 v[24:27], v2 offset:0\n\t" \
    "v_add_f32 v57, v57, v61\n\t" \
    "v_mfma_f32_16x16x32_bf16 v[84:87], v[32:35], v[188:191], v[84:87]\n\t" \
    "v_add_f32 v58, v58, v62\n\t" \
    "v_add_f32 v59, v59, v63\n\t" \
    "v_mfma_f32_16x16x32_bf16 v[92:95], v[36:39], v[188:191], v[92:95]\n\t" \
    "ds_read_b128 v[28:31], v2 offset:1024\n\t" \
    "v_mov_b32 v60, v56\n\t" \
    "v_mfma_f32_16x16x32_bf16 v[88:91], v[36:39], v[184:187], v[88:91]\n\t" \
    "v_mov_b32 v61, v57\n\t" \
    "s_waitcnt lgkmcnt(3)\n\t" \
    "v_mfma_f32_16x16x32_bf16 v[96:99], v[40:43], v[184:187], v[96:99]\n\t" \
    "ds_read_b128 v[32:35], v2 offset:2048\n\t" \
    "s_waitcnt lgkmcnt(3)\n\t" \
    "v_mfma_f32_16x16x32_bf16 v[100:103], v[40:43], v[188:191], v[100:103]\n\t" \
    "v_swap_b32 v193, v196\n\t" \
    "v_swap_b32 v195, v198\n\t" \
    "v_mfma_f32_16x16x32_bf16 v[108:111], v[44:47], v[188:191], v[108:111]\n\t" \
    "ds_read_b128 v[36:39], v2 offset:3072\n\t" \
    "v_mov_b32 v62, v58\n\t" \
    "v_mfma_f32_16x16x32_bf16 v[104:107], v[44:47], v[184:187], v[104:107]\n\t" \
    "v_mov_b32 v63, v59\n\t" \
    "s_waitcnt lgkmcnt(2)\n\t" \
    "v_mfma_f32_16x16x32_bf16 v[64:67], v[24:27], v[192:195], v[64:67]\n\t" \
    "ds_read_b128 v[40:43], v2 offset:4096\n\t" \
    "ds_read_b64 v[200:201], v3 offset:57344\n\t" \
    "v_mfma_f32_16x16x32_bf16 v[68:71], v[24:27], v[196:199], v[68:71]\n\t" \
    "ds_read_b64 v[204:205], v3 offset:57856\n\t" \
    "ds_read_b64 v[202:203], v3 offset:58368\n\t" \
    "v_mfma_f32_16x16x32_bf16 v[76:79], v[28:31], v[196:199], v[76:79]\n\t" \
    "ds_read_b128 v[44:47], v2 offset:5120\n\t" \
    "ds_read_b64 v[206:207], v3 offset:58880\n\t" \
    "v_mfma_f32_16x16x32_bf16 v[72:75], v[28:31], v[192:195], v[72:75]\n\t" \
    "v_permlane32_swap_b32 v56, v60\n\t" \
    "s_waitcnt lgkmcnt(6)\n\t" \
    "v_mfma_f32_16x16x32_bf16 v[80:83], v[32:35], v[192:195], v[80:83]\n\t" \
    "ds_read_b128 v[24:27], v2 offset:6144\n\t" \
    "v_permlane32_swap_b32 v57, v61\n\t" \
    "v_mfma_f32_16x16x32_bf16 v[84:87], v[32:35], v[196:199], v[84:87]\n\t" \
    "v_permlane32_swap_b32 v58, v62\n\t" \
    "v_permlane32_swap_b32 v59, v63\n\t" \
    "v_mfma_f32_16x16x32_bf16 v[92:95], v[36:39], v[196:199], v[92:95]\n\t" \
    "ds_read_b128 v[28:31], v2 offset:7168\n\t" \
    "v_add_f32 v56, v56, v60\n\t" \
    "v_mfma_f32_16x16x32_bf16 v[88:91], v[36:39], v[192:195], v[88:91]\n\t" \
    "v_add_f32 v57, v57, v61\n\t" \
    "s_waitcnt lgkmcnt(3)\n\t" \
    "v_mfma_f32_16x16x32_bf16 v[96:99], v[40:43], v[192:195], v[96:99]\n\t" \
    "ds_read_b128 v[32:35], v2 offset:8192\n\t" \
    "s_waitcnt lgkmcnt(3)\n\t" \
    "v_mfma_f32_16x16x32_bf16 v[100:103], v[40:43], v[196:199], v[100:103]\n\t" \
    "v_swap_b32 v201, v204\n\t" \
    "v_swap_b32 v203, v206\n\t" \
    "v_mfma_f32_16x16x32_bf16 v[108:111], v[44:47], v[196:199], v[108:111]\n\t" \
    "ds_read_b128 v[36:39], v2 offset:9216\n\t" \
    "v_add_f32 v58, v58, v62\n\t" \
    "v_mfma_f32_16x16x32_bf16 v[104:107], v[44:47], v[192:195], v[104:107]\n\t" \
    "v_add_f32 v59, v59, v63\n\t" \
    "s_waitcnt lgkmcnt(2)\n\t" \
    "v_mfma_f32_16x16x32_bf16 v[64:67], v[24:27], v[200:203], v[64:67]\n\t" \
    "ds_read_b128 v[40:43], v2 offset:10240\n\t" \
    "s_barrier\n\t" \
    "v_add_u32 v3, 0xffff0000, v3\n\t" \
    "ds_read_b64 v[208:209], v3 offset:0\n\t" \
    "v_mfma_f32_16x16x32_bf16 v[68:71], v[24:27], v[204:207], v[68:71]\n\t" \
    "ds_read_b64 v[212:213], v3 offset:512\n\t" \
    "ds_read_b64 v[210:211], v3 offset:1024\n\t" \
    "v_mfma_f32_16x16x32_bf16 v[76:79], v[28:31], v[204:207], v[76:79]\n\t" \
    "ds_read_b128 v[44:47], v2 offset:11264\n\t" \
    "ds_read_b64 v[214:215], v3 offset:1536\n\t" \
    "v_mfma_f32_16x16x32_bf16 v[72:75], v[28:31], v[200:203], v[72:75]\n\t" \
    "v_mov_b32 v60, v56\n\t" \
    "s_waitcnt lgkmcnt(6)\n\t" \
    "v_mfma_f32_16x16x32_bf16 v[80:83], v[32:35], v[200:203], v[80:83]\n\t" \
    "ds_read_b128 v[24:27], v2 offset:12288\n\t" \
    "v_mov_b32 v61, v57\n\t" \
    "v_mfma_f32_16x16x32_bf16 v[84:87], v[32:35], v[204:207], v[84:87]\n\t" \
    "v_mov_b32 v62, v58\n\t" \
    "v_mov_b32 v63, v59\n\t" \
    "v_mfma_f32_16x16x32_bf16 v[92:95], v[36:39], v[204:207], v[92:95]\n\t" \
    "ds_read_b128 v[28:31], v2 offset:13312\n\t" \
    "v_permlane16_swap_b32 v56, v60\n\t" \
    "v_mfma_f32_16x16x32_bf16 v[88:91], v[36:39], v[200:203], v[88:91]\n\t" \
    "v_permlane16_swap_b32 v57, v61\n\t" \
    "s_waitcnt lgkmcnt(3)\n\t" \
    "v_mfma_f32_16x16x32_bf16 v[96:99], v[40:43], v[200:203], v[96:99]\n\t" \
    "ds_read_b128 v[32:35], v2 offset:14336\n\t" \
    "s_waitcnt lgkmcnt(3)\n\t" \
    "v_mfma_f32_16x16x32_bf16 v[100:103], v[40:43], v[204:207], v[100:103]\n\t" \
    "v_swap_b32 v209, v212\n\t" \
    "v_swap_b32 v211, v214\n\t" \
    "v_mfma_f32_16x16x32_bf16 v[108:111], v[44:47], v[204:207], v[108:111]\n\t" \
    "ds_read_b128 v[36:39], v2 offset:15360\n\t" \
    "v_permlane16_swap_b32 v58, v62\n\t" \
    "v_mfma_f32_16x16x32_bf16 v[104:107], v[44:47], v[200:203], v[104:107]\n\t" \
    "v_permlane16_swap_b32 v59, v63\n\t" \
    "s_waitcnt lgkmcnt(2)\n\t" \
    "v_mfma_f32_16x16x32_bf16 v[64:67], v[24:27], v[208:211], v[64:67]\n\t" \
    "v_add_u32 v2, 0xfffe0000, v2\n\t" \
    "ds_read_b128 v[40:43], v2 offset:16384\n\t" \
    "ds_read_b64 v[216:217], v3 offset:8192\n\t" \
    "v_mfma_f32_16x16x32_bf16 v[68:71], v[24:27], v[212:215], v[68:71]\n\t" \
    "ds_read_b64 v[220:221], v3 offset:8704\n\t" \
    "ds_read_b64 v[218:219], v3 offset:9216\n\t" \
    "v_mfma_f32_16x16x32_bf16 v[76:79], v[28:31], v[212:215], v[76:79]\n\t" \
    "ds_read_b128 v[44:47], v2 offset:17408\n\t" \
    "ds_read_b64 v[222:223], v3 offset:9728\n\t" \
    "v_mfma_f32_16x16x32_bf16 v[72:75], v[28:31], v[208:211], v[72:75]\n\t" \
    "v_add_f32 v56, v56, v60\n\t" \
    "s_waitcnt lgkmcnt(6)\n\t" \
    "v_mfma_f32_16x16x32_bf16 v[80:83], v[32:35], v[208:211], v[80:83]\n\t" \
    "ds_read_b128 v[24:27], v2 offset:18432\n\t" \
    "v_add_f32 v57, v57, v61\n\t" \
    "v_mfma_f32_16x16x32_bf16 v[84:87], v[32:35], v[212:215], v[84:87]\n\t" \
    "v_add_f32 v58, v58, v62\n\t" \
    "v_add_f32 v59, v59, v63\n\t" \
    "v_mfma_f32_16x16x32_bf16 v[92:95], v[36:39], v[212:215], v[92:95]\n\t" \
    "ds_read_b128 v[28:31], v2 offset:19456\n\t" \
    "ds_write_b128 v17, v[56:59] offset:0\n\t" \
    "v_mfma_f32_16x16x32_bf16 v[88:91], v[36:39], v[208:211], v[88:91]\n\t" \
    "s_waitcnt lgkmcnt(4)\n\t" \
    "v_mfma_f32_16x16x32_bf16 v[96:99], v[40:43], v[208:211], v[96:99]\n\t" \
    "ds_read_b128 v[32:35], v2 offset:20480\n\t" \
    "s_waitcnt lgkmcnt(4)\n\t" \
    "v_mfma_f32_16x16x32_bf16 v[100:103], v[40:43], v[212:215], v[100:103]\n\t" \
    "v_swap_b32 v217, v220\n\t" \
    "v_swap_b32 v219, v222\n\t" \
    "v_mfma_f32_16x16x32_bf16 v[108:111], v[44:47], v[212:215], v[108:111]\n\t" \
    "ds_read_b128 v[36:39], v2 offset:21504\n\t" \
    "v_mfma_f32_16x16x32_bf16 v[104:107], v[44:47], v[208:211], v[104:107]\n\t" \
    "s_waitcnt lgkmcnt(3)\n\t" \
    "v_mfma_f32_16x16x32_bf16 v[64:67], v[24:27], v[216:219], v[64:67]\n\t" \
    "ds_read_b128 v[40:43], v2 offset:22528\n\t" \
    "s_barrier\n\t" \
    "ds_read_b64 v[224:225], v3 offset:32768\n\t" \
    "v_mfma_f32_16x16x32_bf16 v[68:71], v[24:27], v[220:223], v[68:71]\n\t" \
    "ds_read_b64 v[228:229], v3 offset:33280\n\t" \
    "ds_read_b64 v[226:227], v3 offset:33792\n\t" \
    "v_mfma_f32_16x16x32_bf16 v[76:79], v[28:31], v[220:223], v[76:79]\n\t" \
    "ds_read_b128 v[44:47], v2 offset:23552\n\t" \
    "ds_read_b64 v[230:231], v3 offset:34304\n\t" \
    "v_mfma_f32_16x16x32_bf16 v[72:75], v[28:31], v[216:219], v[72:75]\n\t" \
    "s_waitcnt lgkmcnt(6)\n\t" \
    "v_mfma_f32_16x16x32_bf16 v[80:83], v[32:35], v[216:219], v[80:83]\n\t" \
    "ds_read_b128 v[24:27], v2 offset:24576\n\t" \
    "v_mfma_f32_16x16x32_bf16 v[84:87], v[32:35], v[220:223], v[84:87]\n\t" \
    "v_mfma_f32_16x16x32_bf16 v[92:95], v[36:39], v[220:223], v[92:95]\n\t" \
    "ds_read_b128 v[28:31], v2 offset:25600\n\t" \
    "v_mfma_f32_16x16x32_bf16 v[88:91], v[36:39], v[216:219], v[88:91]\n\t" \
    "s_waitcnt lgkmcnt(3)\n\t" \
    "v_mfma_f32_16x16x32_bf16 v[96:99], v[40:43], v[216:219], v[96:99]\n\t" \
    "ds_read_b128 v[32:35], v2 offset:26624\n\t" \
    "s_waitcnt lgkmcnt(3)\n\t" \
    "v_mfma_f32_16x16x32_bf16 v[100:103], v[40:43], v[220:223], v[100:103]\n\t" \
    "v_swap_b32 v225, v228\n\t" \
    "v_swap_b32 v227, v230\n\t" \
    "v_mfma_f32_16x16x32_bf16 v[108:111], v[44:47], v[220:223], v[108:111]\n\t" \
    "ds_read_b128 v[36:39], v2 offset:27648\n\t" \
    "v_mfma_f32_16x16x32_bf16 v[104:107], v[44:47], v[216:219], v[104:107]\n\t" \
    "s_waitcnt lgkmcnt(2)\n\t" \
    "v_mfma_f32_16x16x32_bf16 v[64:67], v[24:27], v[224:227], v[64:67]\n\t" \
    "ds_read_b128 v[40:43], v2 offset:28672\n\t" \
    "ds_read_b64 v[232:233], v3 offset:40960\n\t" \
    "v_mfma_f32_16x16x32_bf16 v[68:71], v[24:27], v[228:231], v[68:71]\n\t" \
    "ds_read_b64 v[236:237], v3 offset:41472\n\t" \
    "ds_read_b64 v[234:235], v3 offset:41984\n\t" \
    "v_mfma_f32_16x16x32_bf16 v[76:79], v[28:31], v[228:231], v[76:79]\n\t" \
    "ds_read_b128 v[44:47], v2 offset:29696\n\t" \
    "ds_read_b64 v[238:239], v3 offset:42496\n\t" \
    "v_mfma_f32_16x16x32_bf16 v[72:75], v[28:31], v[224:227], v[72:75]\n\t" \
    "s_waitcnt lgkmcnt(6)\n\t" \
    "v_mfma_f32_16x16x32_bf16 v[80:83], v[32:35], v[224:227], v[80:83]\n\t" \
    "ds_read_b128 v[24:27], v2 offset:30720\n\t" \
    "v_mfma_f32_16x16x32_bf16 v[84:87], v[32:35], v[228:231], v[84:87]\n\t" \
    "v_mfma_f32_16x16x32_bf16 v[92:95], v[36:39], v[228:231], v[92:95]\n\t" \
    "ds_read_b128 v[28:31], v2 offset:31744\n\t" \
    "v_mfma_f32_16x16x32_bf16 v[88:91], v[36:39], v[224:227], v[88:91]\n\t" \
    "s_waitcnt lgkmcnt(3)\n\t" \
    "v_mfma_f32_16x16x32_bf16 v[96:99], v[40:43], v[224:227], v[96:99]\n\t" \
    "ds_read_b128 v[32:35], v2 offset:49152\n\t" \
    "s_waitcnt lgkmcnt(3)\n\t" \
    "v_mfma_f32_16x16x32_bf16 v[100:103], v[40:43], v[228:231], v[100:103]\n\t" \
    "v_swap_b32 v233, v236\n\t" \
    "v_swap_b32 v235, v238\n\t" \
    "v_mfma_f32_16x16x32_bf16 v[108:111], v[44:47], v[228:231], v[108:111]\n\t" \
    "ds_read_b128 v[36:39], v2 offset:50176\n\t" \
    "v_mfma_f32_16x16x32_bf16 v[104:107], v[44:47], v[224:227], v[104:107]\n\t" \
    "s_waitcnt lgkmcnt(2)\n\t" \
    "v_mfma_f32_16x16x32_bf16 v[64:67], v[24:27], v[232:235], v[64:67]\n\t" \
    "ds_read_b128 v[40:43], v2 offset:51200\n\t" \
    "s_barrier\n\t" \
    "v_add_u32 v3, 0x10000, v3\n\t" \
    "ds_read_b64 v[240:241], v3 offset:0\n\t" \
    "v_mfma_f32_16x16x32_bf16 v[68:71], v[24:27], v[236:239], v[68:71]\n\t" \
    "ds_read_b64 v[244:245], v3 offset:512\n\t" \
    "ds_read_b64 v[242:243], v3 offset:1024\n\t" \
    "v_mfma_f32_16x16x32_bf16 v[76:79], v[28:31], v[236:239], v[76:79]\n\t" \
    "ds_read_b128 v[44:47], v2 offset:52224\n\t" \
    "ds_read_b64 v[246:247], v3 offset:1536\n\t" \
    "v_mfma_f32_16x16x32_bf16 v[72:75], v[28:31], v[232:235], v[72:75]\n\t" \
    "s_waitcnt lgkmcnt(6)\n\t" \
    "v_mfma_f32_16x16x32_bf16 v[80:83], v[32:35], v[232:235], v[80:83]\n\t" \
    "ds_read_b128 v[24:27], v2 offset:53248\n\t" \
    "s_lshl_b32 s50, %1, 11\n\t" \
    "v_mfma_f32_16x16x32_bf16 v[84:87], v[32:35], v[236:239], v[84:87]\n\t" \
    "s_add_u32 s50, s50, 0x24400\n\t" \
    "v_add_u32 v17, s50, v18\n\t" \
    "v_mfma_f32_16x16x32_bf16 v[92:95], v[36:39], v[236:239], v[92:95]\n\t" \
    "ds_read_b128 v[28:31], v2 offset:54272\n\t" \
    "ds_read_b128 v[48:51], v17 offset:0\n\t" \
    "v_mfma_f32_16x16x32_bf16 v[88:91], v[36:39], v[232:235], v[88:91]\n\t" \
    "ds_read_b128 v[52:55], v17 offset:256\n\t" \
    "s_waitcnt lgkmcnt(5)\n\t" \
    "v_mfma_f32_16x16x32_bf16 v[96:99], v[40:43], v[232:235], v[96:99]\n\t" \
    "ds_read_b128 v[32:35], v2 offset:55296\n\t" \
    "s_waitcnt lgkmcnt(5)\n\t" \
    "v_mfma_f32_16x16x32_bf16 v[100:103], v[40:43], v[236:239], v[100:103]\n\t" \
    "v_swap_b32 v241, v244\n\t" \
    "v_swap_b32 v243, v246\n\t" \
    "v_mfma_f32_16x16x32_bf16 v[108:111], v[44:47], v[236:239], v[108:111]\n\t" \
    "ds_read_b128 v[36:39], v2 offset:56320\n\t" \
    "s_nop 0\n\t" \
    "v_mfma_f32_16x16x32_bf16 v[104:107], v[44:47], v[232:235], v[104:107]\n\t" \
    "s_nop 0\n\t" \
    "s_waitcnt lgkmcnt(4)\n\t" \
    "v_mfma_f32_16x16x32_bf16 v[64:67], v[24:27], v[240:243], v[64:67]\n\t" \
    "ds_read_b128 v[40:43], v2 offset:57344\n\t" \
    "ds_read_b64 v[248:249], v3 offset:8192\n\t" \
    "v_mfma_f32_16x16x32_bf16 v[68:71], v[24:27], v[244:247], v[68:71]\n\t" \
    "ds_read_b64 v[252:253], v3 offset:8704\n\t" \
    "ds_read_b64 v[250:251], v3 offset:9216\n\t" \
    "v_mfma_f32_16x16x32_bf16 v[76:79], v[28:31], v[244:247], v[76:79]\n\t" \
    "ds_read_b128 v[44:47], v2 offset:58368\n\t" \
    "ds_read_b64 v[254:255], v3 offset:9728\n\t" \
    "v_mfma_f32_16x16x32_bf16 v[72:75], v[28:31], v[240:243], v[72:75]\n\t" \
    "s_waitcnt lgkmcnt(8)\n\t" \
    "s_waitcnt lgkmcnt(6)\n\t" \
    "v_mfma_f32_16x16x32_bf16 v[80:83], v[32:35], v[240:243], v[80:83]\n\t" \
    "ds_read_b128 v[24:27], v2 offset:59392\n\t" \
    "v_max3_u32 v17, v48, v49, v50\n\t" \
    "v_mfma_f32_16x16x32_bf16 v[84:87], v[32:35], v[244:247], v[84:87]\n\t" \
    "v_max3_u32 v17, v51, v52, v17\n\t" \
    "v_max3_u32 v17, v53, v54, v17\n\t" \
    "v_mfma_f32_16x16x32_bf16 v[92:95], v[36:39], v[244:247], v[92:95]\n\t" \
    "ds_read_b128 v[28:31], v2 offset:60416\n\t" \
    "v_max_u32 v17, v55, v17\n\t" \
    "v_mfma_f32_16x16x32_bf16 v[88:91], v[36:39], v[240:243], v[88:91]\n\t" \
    "v_cmp_eq_u32 vcc, -1, v17\n\t" \
    "s_and_b64 vcc, exec, vcc\n\t" \
    "s_cbranch_vccnz .Lres_%=\n\t" \
    ".Loks_%=:\n\t" \
    "s_waitcnt lgkmcnt(3)\n\t" \
    "v_mfma_f32_16x16x32_bf16 v[96:99], v[40:43], v[240:243], v[96:99]\n\t" \
    "ds_read_b128 v[32:35], v2 offset:61440\n\t" \
    "s_waitcnt lgkmcnt(3)\n\t" \
    "v_mfma_f32_16x16x32_bf16 v[100:103], v[40:43], v[244:247], v[100:103]\n\t" \
    "v_swap_b32 v249, v252\n\t" \
    "v_swap_b32 v251, v254\n\t" \
    "v_mfma_f32_16x16x32_bf16 v[108:111], v[44:47], v[244:247], v[108:111]\n\t" \
    "ds_read_b128 v[36:39], v2 offset:62464\n\t" \
    "v_add_f32 v4, v48, v52\n\t" \
    "v_mfma_f32_16x16x32_bf16 v[104:107], v[44:47], v[240:243], v[104:107]\n\t" \
    "v_add_f32 v5, v49, v53\n\t" \
    "s_waitcnt lgkmcnt(2)\n\t" \
    "v_mfma_f32_16x16x32_bf16 v[64:67], v[24:27], v[248:251], v[64:67]\n\t" \
    "ds_read_b128 v[40:43], v2 offset:63488\n\t" \
    "v_add_f32 v6, v50, v54\n\t" \
    "v_mfma_f32_16x16x32_bf16 v[68:71], v[24:27], v[252:255], v[68:71]\n\t" \
    "v_add_f32 v7, v51, v55\n\t" \
    "v_mfma_f32_16x16x32_bf16 v[76:79], v[28:31], v[252:255], v[76:79]\n\t" \
    "ds_read_b128 v[44:47], v2 offset:64512\n\t" \
    "ds_read_b128 v[8:11], v16 offset:0\n\t" \
    "v_mfma_f32_16x16x32_bf16 v[72:75], v[28:31], v[248:251], v[72:75]\n\t" \
    "v_mov_b32 v12, v4\n\t" \
    "s_waitcnt lgkmcnt(3)\n\t" \
    "v_mfma_f32_16x16x32_bf16 v[80:83], v[32:35], v[248:251], v[80:83]\n\t" \
    "s_barrier\n\t" \
    "v_add_u32 v2, 0x10000, v2\n\t" \
    "ds_read_b128 v[24:27], v2 offset:32768\n\t" \
    "v_mov_b32 v13, v5\n\t" \
    "v_mfma_f32_16x16x32_bf16 v[84:87], v[32:35], v[252:255], v[84:87]\n\t" \
    "v_mov_b32 v14, v6\n\t" \
    "v_mov_b32 v15, v7\n\t" \
    "v_mfma_f32_16x16x32_bf16 v[92:95], v[36:39], v[252:255], v[92:95]\n\t" \
    "ds_read_b128 v[28:31], v2 offset:33792\n\t" \
    "v_permlane32_swap_b32 v4, v12\n\t" \
    "v_mfma_f32_16x16x32_bf16 v[88:91], v[36:39], v[248:251], v[88:91]\n\t" \
    "v_permlane32_swap_b32 v5, v13\n\t" \
    "s_waitcnt lgkmcnt(3)\n\t" \
    "v_mfma_f32_16x16x32_bf16 v[96:99], v[40:43], v[248:251], v[96:99]\n\t" \
    "ds_read_b128 v[32:35], v2 offset:34816\n\t" \
    "v_permlane32_swap_b32 v6, v14\n\t" \
    "v_mfma_f32_16x16x32_bf16 v[100:103], v[40:43], v[252:255], v[100:103]\n\t" \
    "v_permlane32_swap_b32 v7, v15\n\t" \
    "v_add_f32 v4, v4, v12\n\t" \
    "v_mfma_f32_16x16x32_bf16 v[108:111], v[44:47], v[252:255], v[108:111]\n\t" \
    "ds_read_b128 v[36:39], v2 offset:35840\n\t" \
    "v_add_f32 v5, v5, v13\n\t" \
    "v_mfma_f32_16x16x32_bf16 v[104:107], v[44:47], v[248:251], v[104:107]\n\t" \
    "v_add_f32 v6, v6, v14\n\t" \
    "s_waitcnt lgkmcnt(2)\n\t" \
    "v_mfma_f32_16x16x32_bf16 v[112:115], v[24:27], v[128:131], 0\n\t" \
    "ds_read_b128 v[40:43], v2 offset:36864\n\t" \
    "v_add_f32 v7, v7, v15\n\t" \
    "v_mfma_f32_16x16x32_bf16 v[116:119], v[24:27], v[132:135], 0\n\t" \
    "v_mov_b32 v12, v4\n\t" \
    "v_mfma_f32_16x16x32_bf16 v[124:127], v[28:31], v[132:135], 0\n\t" \
    "ds_read_b128 v[44:47], v2 offset:37888\n\t" \
    "v_mov_b32 v13, v5\n\t" \
    "v_mfma_f32_16x16x32_bf16 v[120:123], v[28:31], v[128:131], 0\n\t" \
    "v_mov_b32 v14, v6\n\t" \
    "s_waitcnt lgkmcnt(2)\n\t" \
    "v_mfma_f32_16x16x32_bf16 v[112:115], v[32:35], v[136:139], v[112:115]\n\t" \
    "ds_read_b128 v[24:27], v2 offset:38912\n\t" \
    "v_mov_b32 v15, v7\n\t" \
    "v_mfma_f32_16x16x32_bf16 v[116:119], v[32:35], v[140:143], v[116:119]\n\t" \
    "v_permlane16_swap_b32 v4, v12\n\t" \
    "v_mfma_f32_16x16x32_bf16 v[124:127], v[36:39], v[140:143], v[124:127]\n\t" \
    "ds_read_b128 v[28:31], v2 offset:39936\n\t" \
    "v_permlane16_swap_b32 v5, v13\n\t" \
    "v_mfma_f32_16x16x32_bf16 v[120:123], v[36:39], v[136:139], v[120:123]\n\t" \
    "v_permlane16_swap_b32 v6, v14\n\t" \
    "s_waitcnt lgkmcnt(2)\n\t" \
    "v_mfma_f32_16x16x32_bf16 v[112:115], v[40:43], v[144:147], v[112:115]\n\t" \
    "ds_read_b128 v[32:35], v2 offset:40960\n\t" \
    "v_permlane16_swap_b32 v7, v15\n\t" \
    "v_mfma_f32_16x16x32_bf16 v[116:119], v[40:43], v[148:151], v[116:119]\n\t" \
    "v_add_f32 v4, v4, v12\n\t" \
    "v_add_f32 v5, v5, v13\n\t" \
    "v_mfma_f32_16x16x32_bf16 v[124:127], v[44:47], v[148:151], v[124:127]\n\t" \
    "ds_read_b128 v[36:39], v2 offset:41984\n\t" \
    "v_add_f32 v6, v6, v14\n\t" \
    "v_mfma_f32_16x16x32_bf16 v[120:123], v[44:47], v[144:147], v[120:123]\n\t" \
    "v_add_f32 v7, v7, v15\n\t" \
    "s_waitcnt lgkmcnt(2)\n\t" \
    "v_mfma_f32_16x16x32_bf16 v[112:115], v[24:27], v[152:155], v[112:115]\n\t" \
    "ds_read_b128 v[40:43], v2 offset:43008\n\t" \
    "v_mul_f32 v4, 0xbb000000, v4\n\t" \
    "v_mfma_f32_16x16x32_bf16 v[116:119], v[24:27], v[156:159], v[116:119]\n\t" \
    "v_mul_f32 v6, 0xbb000000, v6\n\t" \
    "v_mul_f32 v5, 0x3b000000, v5\n\t" \
    "v_mfma_f32_16x16x32_bf16 v[124:127], v[28:31], v[156:159], v[124:127]\n\t" \
    "ds_read_b128 v[44:47], v2 offset:44032\n\t" \
    "v_mul_f32 v7, 0x3b000000, v7\n\t" \
    "v_mfma_f32_16x16x32_bf16 v[120:123], v[28:31], v[152:155], v[120:123]\n\t" \
    "v_fma_f32 v5, -v4, v4, v5\n\t" \
    "s_waitcnt lgkmcnt(2)\n\t" \
    "v_mfma_f32_16x16x32_bf16 v[112:115], v[32:35], v[160:163], v[112:115]\n\t" \
    "ds_read_b128 v[24:27], v2 offset:45056\n\t" \
    "v_fma_f32 v7, -v6, v6, v7\n\t" \
    "v_mfma_f32_16x16x32_bf16 v[116:119], v[32:35], v[164:167], v[116:119]\n\t" \
    "v_max_f32 v5, 0, v5\n\t" \
    "v_mfma_f32_16x16x32_bf16 v[124:127], v[36:39], v[164:167], v[124:127]\n\t" \
    "ds_read_b128 v[28:31], v2 offset:46080\n\t" \
    "v_max_f32 v7, 0, v7\n\t" \
    "v_mfma_f32_16x16x32_bf16 v[120:123], v[36:39], v[160:163], v[120:123]\n\t" \
    "v_cvt_pk_bf16_f32 v12, v5, v7\n\t" \
    "s_waitcnt lgkmcnt(2)\n\t" \
    "v_mfma_f32_16x16x32_bf16 v[112:115], v[40:43], v[168:171], v[112:115]\n\t" \
    "ds_read_b128 v[32:35], v2 offset:47104\n\t" \
    "v_lshlrev_b32 v5, 16, v12\n\t" \
    "v_mfma_f32_16x16x32_bf16 v[116:119], v[40:43], v[172:175], v[116:119]\n\t" \
    "v_and_b32 v7, 0xffff0000, v12\n\t" \
    "v_add_f32 v5, 0x3727c5ac, v5\n\t" \
    "v_mfma_f32_16x16x32_bf16 v[124:127], v[44:47], v[172:175], v[124:127]\n\t" \
    "ds_read_b128 v[36:39], v2 offset:48128\n\t" \
    "v_add_f32 v7, 0x3727c5ac, v7\n\t" \
    "v_mfma_f32_16x16x32_bf16 v[120:123], v[44:47], v[168:171], v[120:123]\n\t" \
    "v_cvt_pk_bf16_f32 v12, v5, v7\n\t" \
    "s_waitcnt lgkmcnt(2)\n\t" \
    "v_mfma_f32_16x16x32_bf16 v[112:115], v[24:27], v[176:179], v[112:115]\n\t" \
    "s_barrier\n\t" \
    "ds_read_b128 v[40:43], v2 offset:16384\n\t" \
    "v_lshlrev_b32 v5, 16, v12\n\t" \
    "v_mfma_f32_16x16x32_bf16 v[116:119], v[24:27], v[180:183], v[116:119]\n\t" \
    "v_and_b32 v7, 0xffff0000, v12\n\t" \
    "v_mfma_f32_16x16x32_bf16 v[124:127], v[28:31], v[180:183], v[124:127]\n\t" \
    "ds_read_b128 v[44:47], v2 offset:17408\n\t" \
    "v_sqrt_f32 v5, v5\n\t" \
    "v_mfma_f32_16x16x32_bf16 v[120:123], v[28:31], v[176:179], v[120:123]\n\t" \
    "v_sqrt_f32 v7, v7\n\t" \
    "s_waitcnt lgkmcnt(2)\n\t" \
    "v_mfma_f32_16x16x32_bf16 v[112:115], v[32:35], v[184:187], v[112:115]\n\t" \
    "ds_read_b128 v[24:27], v2 offset:18432\n\t" \
    "v_cvt_pk_bf16_f32 v12, v5, v7\n\t" \
    "v_mfma_f32_16x16x32_bf16 v[116:119], v[32:35], v[188:191], v[116:119]\n\t" \
    "v_lshlrev_b32 v5, 16, v12\n\t" \
    "v_and_b32 v7, 0xffff0000, v12\n\t" \
    "v_mfma_f32_16x16x32_bf16 v[124:127], v[36:39], v[188:191], v[124:127]\n\t" \
    "ds_read_b128 v[28:31], v2 offset:19456\n\t" \
    "ds_read_b128 v[12:15], v16 offset:64\n\t" \
    "v_mfma_f32_16x16x32_bf16 v[120:123], v[36:39], v[184:187], v[120:123]\n\t" \
    "v_rcp_f32 v5, v5\n\t" \
    "s_waitcnt lgkmcnt(3)\n\t" \
    "v_mfma_f32_16x16x32_bf16 v[112:115], v[40:43], v[192:195], v[112:115]\n\t" \
    "ds_read_b128 v[32:35], v2 offset:20480\n\t" \
    "v_rcp_f32 v7, v7\n\t" \
    "v_mfma_f32_16x16x32_bf16 v[116:119], v[40:43], v[196:199], v[116:119]\n\t" \
    "v_mul_f32 v5, v5, v5\n\t" \
    "v_mul_f32 v7, v7, v7\n\t" \
    "v_mfma_f32_16x16x32_bf16 v[124:127], v[44:47], v[196:199], v[124:127]\n\t" \
    "ds_read_b128 v[36:39], v2 offset:21504\n\t" \
    "v_fmac_f32 v64, v4, v8\n\t" \
    "v_mfma_f32_16x16x32_bf16 v[120:123], v[44:47], v[192:195], v[120:123]\n\t" \
    "v_fmac_f32 v65, v4, v9\n\t" \
    "s_waitcnt lgkmcnt(3)\n\t" \
    "v_mfma_f32_16x16x32_bf16 v[112:115], v[24:27], v[200:203], v[112:115]\n\t" \
    "ds_read_b128 v[40:43], v2 offset:22528\n\t" \
    "v_fmac_f32 v66, v4, v10\n\t" \
    "v_mfma_f32_16x16x32_bf16 v[116:119], v[24:27], v[204:207], v[116:119]\n\t" \
    "v_fmac_f32 v67, v4, v11\n\t" \
    "v_mfma_f32_16x16x32_bf16 v[124:127], v[28:31], v[204:207], v[124:127]\n\t" \
    "ds_read_b128 v[44:47], v2 offset:23552\n\t" \
    "v_fmac_f32 v68, v6, v8\n\t" \
    "v_mfma_f32_16x16x32_bf16 v[120:123], v[28:31], v[200:203], v[120:123]\n\t" \
    "v_fmac_f32 v69, v6, v9\n\t" \
    "s_waitcnt lgkmcnt(2)\n\t" \
    "v_mfma_f32_16x16x32_bf16 v[112:115], v[32:35], v[208:211], v[112:115]\n\t" \
    "ds_read_b128 v[24:27], v2 offset:24576\n\t" \
    "v_fmac_f32 v70, v6, v10\n\t" \
    "v_mfma_f32_16x16x32_bf16 v[116:119], v[32:35], v[212:215], v[116:119]\n\t" \
    "v_fmac_f32 v71, v6, v11\n\t" \
    "v_mfma_f32_16x16x32_bf16 v[124:127], v[36:39], v[212:215], v[124:127]\n\t" \
    "ds_read_b128 v[28:31], v2 offset:25600\n\t" \
    "ds_read_b128 v[8:11], v16 offset:128\n\t" \
    "v_mfma_f32_16x16x32_bf16 v[120:123], v[36:39], v[208:211], v[120:123]\n\t" \
    "v_fmac_f32 v72, v4, v12\n\t" \
    "s_waitcnt lgkmcnt(3)\n\t" \
    "v_mfma_f32_16x16x32_bf16 v[112:115], v[40:43], v[216:219], v[112:115]\n\t" \
    "ds_read_b128 v[32:35], v2 offset:26624\n\t" \
    "v_fmac_f32 v73, v4, v13\n\t" \
    "v_mfma_f32_16x16x32_bf16 v[116:119], v[40:43], v[220:223], v[116:119]\n\t" \
    "v_fmac_f32 v74, v4, v14\n\t" \
    "v_mfma_f32_16x16x32_bf16 v[124:127], v[44:47], v[220:223], v[124:127]\n\t" \
    "ds_read_b128 v[36:39], v2 offset:27648\n\t" \
    "v_fmac_f32 v75, v4, v15\n\t" \
    "v_mfma_f32_16x16x32_bf16 v[120:123], v[44:47], v[216:219], v[120:123]\n\t" \
    "v_fmac_f32 v76, v6, v12\n\t" \
    "s_waitcnt lgkmcnt(3)\n\t" \
    "v_mfma_f32_16x16x32_bf16 v[112:115], v[24:27], v[224:227], v[112:115]\n\t" \
    "ds_read_b128 v[40:43], v2 offset:28672\n\t" \
    "v_fmac_f32 v77, v6, v13\n\t" \
    "v_mfma_f32_16x16x32_bf16 v[116:119], v[24:27], v[228:231], v[116:119]\n\t" \
    "v_fmac_f32 v78, v6, v14\n\t" \
    "v_fmac_f32 v79, v6, v15\n\t" \
    "v_mfma_f32_16x16x32_bf16 v[124:127], v[28:31], v[228:231], v[124:127]\n\t" \
    "ds_read_b128 v[44:47], v2 offset:29696\n\t" \
    "ds_read_b128 v[12:15], v16 offset:192\n\t" \
    "v_mfma_f32_16x16x32_bf16 v[120:123], v[28:31], v[224:227], v[120:123]\n\t" \
    "s_add_u32 s61, s45, 0x0\n\t" \
    "s_waitcnt lgkmcnt(3)\n\t" \
    "v_mfma_f32_16x16x32_bf16 v[112:115], v[32:35], v[232:235], v[112:115]\n\t" \
    "ds_read_b128 v[24:27], v2 offset:30720\n\t" \
    "v_mul_f32 v64, v64, v72\n\t" \
    "v_mfma_f32_16x16x32_bf16 v[116:119], v[32:35], v[236:239], v[116:119]\n\t" \
    "v_mul_f32 v65, v65, v73\n\t" \
    "v_mul_f32 v66, v66, v74\n\t" \
    "v_mfma_f32_16x16x32_bf16 v[124:127], v[36:39], v[236:239], v[124:127]\n\t" \
    "ds_read_b128 v[28:31], v2 offset:31744\n\t" \
    "v_mul_f32 v67, v67, v75\n\t" \
    "v_mfma_f32_16x16x32_bf16 v[120:123], v[36:39], v[232:235], v[120:123]\n\t" \
    "v_mul_f32 v68, v68, v76\n\t" \
    "s_waitcnt lgkmcnt(3)\n\t" \
    "v_mfma_f32_16x16x32_bf16 v[112:115], v[40:43], v[240:243], v[112:115]\n\t" \
    "s_barrier\n\t" \
    "ds_read_b128 v[32:35], v2 offset:49152\n\t" \
    "v_mul_f32 v69, v69, v77\n\t" \
    "v_mfma_f32_16x16x32_bf16 v[116:119], v[40:43], v[244:247], v[116:119]\n\t" \
    "v_mul_f32 v70, v70, v78\n\t" \
    "v_mfma_f32_16x16x32_bf16 v[124:127], v[44:47], v[244:247], v[124:127]\n\t" \
    "ds_read_b128 v[36:39], v2 offset:50176\n\t" \
    "v_mul_f32 v71, v71, v79\n\t" \
    "v_mfma_f32_16x16x32_bf16 v[120:123], v[44:47], v[240:243], v[120:123]\n\t" \
    "v_mul_f32 v64, v64, v5\n\t" \
    "s_waitcnt lgkmcnt(2)\n\t" \
    "v_mfma_f32_16x16x32_bf16 v[112:115], v[24:27], v[248:251], v[112:115]\n\t" \
    "ds_read_b128 v[40:43], v2 offset:51200\n\t" \
    "v_mul_f32 v65, v65, v5\n\t" \
    "v_mfma_f32_16x16x32_bf16 v[116:119], v[24:27], v[252:255], v[116:119]\n\t" \
    "v_mul_f32 v66, v66, v5\n\t" \
    "v_mul_f32 v67, v67, v5\n\t" \
    "v_mfma_f32_16x16x32_bf16 v[124:127], v[28:31], v[252:255], v[124:127]\n\t" \
    "ds_read_b128 v[44:47], v2 offset:52224\n\t" \
    "v_mul_f32 v68, v68, v7\n\t" \
    "v_mfma_f32_16x16x32_bf16 v[120:123], v[28:31], v[248:251], v[120:123]\n\t" \
    "v_mul_f32 v69, v69, v7\n\t" \
    "s_waitcnt lgkmcnt(2)\n\t" \
    "v_mfma_f32_16x16x32_bf16 v[48:51], v[32:35], v[128:131], 0\n\t" \
    "ds_read_b128 v[24:27], v2 offset:53248\n\t" \
    "v_mul_f32 v70, v70, v7\n\t" \
    "v_mfma_f32_16x16x32_bf16 v[52:55], v[32:35], v[132:135], 0\n\t" \
    "v_mul_f32 v71, v71, v7\n\t" \
    "v_mfma_f32_16x16x32_bf16 v[60:63], v[36:39], v[132:135], 0\n\t" \
    "ds_read_b128 v[28:31], v2 offset:54272\n\t" \
    "v_cvt_pk_bf16_f32 v72, v64, v65\n\t" \
    "v_mfma_f32_16x16x32_bf16 v[56:59], v[36:39], v[128:131], 0\n\t" \
    "v_cvt_pk_bf16_f32 v73, v66, v67\n\t" \
    "s_waitcnt lgkmcnt(2)\n\t" \
    "v_mfma_f32_16x16x32_bf16 v[48:51], v[40:43], v[136:139], v[48:51]\n\t" \
    "ds_read_b128 v[32:35], v2 offset:55296\n\t" \
    "v_cvt_pk_bf16_f32 v74, v68, v69\n\t" \
    "v_mfma_f32_16x16x32_bf16 v[52:55], v[40:43], v[140:143], v[52:55]\n\t" \
    "v_cvt_pk_bf16_f32 v75, v70, v71\n\t" \
    "v_permlane16_swap_b32 v72, v74\n\t" \
    "v_mfma_f32_16x16x32_bf16 v[60:63], v[44:47], v[140:143], v[60:63]\n\t" \
    "ds_read_b128 v[36:39], v2 offset:56320\n\t" \
    "v_permlane16_swap_b32 v73, v75\n\t" \
    "v_mfma_f32_16x16x32_bf16 v[56:59], v[44:47], v[136:139], v[56:59]\n\t" \
    "buffer_store_dwordx4 v[72:75], v1, s[36:39], s61 offen sc1\n\t" \
    "s_waitcnt lgkmcnt(2)\n\t" \
    "v_mfma_f32_16x16x32_bf16 v[48:51], v[24:27], v[144:147], v[48:51]\n\t" \
    "ds_read_b128 v[40:43], v2 offset:57344\n\t" \
    "v_fmac_f32 v80, v4, v8\n\t" \
    "v_mfma_f32_16x16x32_bf16 v[52:55], v[24:27], v[148:151], v[52:55]\n\t" \
    "v_fmac_f32 v81, v4, v9\n\t" \
    "v_fmac_f32 v82, v4, v10\n\t" \
    "v_mfma_f32_16x16x32_bf16 v[60:63], v[28:31], v[148:151], v[60:63]\n\t" \
    "ds_read_b128 v[44:47], v2 offset:58368\n\t" \
    "v_fmac_f32 v83, v4, v11\n\t" \
    "v_mfma_f32_16x16x32_bf16 v[56:59], v[28:31], v[144:147], v[56:59]\n\t" \
    "v_fmac_f32 v84, v6, v8\n\t" \
    "s_waitcnt lgkmcnt(2)\n\t" \
    "v_mfma_f32_16x16x32_bf16 v[48:51], v[32:35], v[152:155], v[48:51]\n\t" \
    "ds_read_b128 v[24:27], v2 offset:59392\n\t" \
    "v_fmac_f32 v85, v6, v9\n\t" \
    "v_mfma_f32_16x16x32_bf16 v[52:55], v[32:35], v[156:159], v[52:55]\n\t" \
    "v_fmac_f32 v86, v6, v10\n\t" \
    "v_mfma_f32_16x16x32_bf16 v[60:63], v[36:39], v[156:159], v[60:63]\n\t" \
    "ds_read_b128 v[28:31], v2 offset:60416\n\t" \
    "v_fmac_f32 v87, v6, v11\n\t" \
    "v_mfma_f32_16x16x32_bf16 v[56:59], v[36:39], v[152:155], v[56:59]\n\t" \
    "ds_read_b128 v[8:11], v16 offset:256\n\t" \
    "s_waitcnt lgkmcnt(3)\n\t" \
    "v_mfma_f32_16x16x32_bf16 v[48:51], v[40:43], v[160:163], v[48:51]\n\t" \
    "ds_read_b128 v[32:35], v2 offset:61440\n\t" \
    "v_fmac_f32 v88, v4, v12\n\t" \
    "v_mfma_f32_16x16x32_bf16 v[52:55], v[40:43], v[164:167], v[52:55]\n\t" \
    "v_fmac_f32 v89, v4, v13\n\t" \
    "v_fmac_f32 v90, v4, v14\n\t" \
    "v_mfma_f32_16x16x32_bf16 v[60:63], v[44:47], v[164:167], v[60:63]\n\t" \
    "ds_read_b128 v[36:39], v2 offset:62464\n\t" \
    "v_fmac_f32 v91, v4, v15\n\t" \
    "v_mfma_f32_16x16x32_bf16 v[56:59], v[44:47], v[160:163], v[56:59]\n\t" \
    "v_fmac_f32 v92, v6, v12\n\t" \
    "s_waitcnt lgkmcnt(3)\n\t" \
    "v_mfma_f32_16x16x32_bf16 v[48:51], v[24:27], v[168:171], v[48:51]\n\t" \
    "ds_read_b128 v[40:43], v2 offset:63488\n\t" \
    "v_fmac_f32 v93, v6, v13\n\t" \
    "v_mfma_f32_16x16x32_bf16 v[52:55], v[24:27], v[172:175], v[52:55]\n\t" \
    "v_fmac_f32 v94, v6, v14\n\t" \
    "v_mfma_f32_16x16x32_bf16 v[60:63], v[28:31], v[172:175], v[60:63]\n\t" \
    "ds_read_b128 v[44:47], v2 offset:64512\n\t" \
    "v_fmac_f32 v95, v6, v15\n\t" \
    "v_mfma_f32_16x16x32_bf16 v[56:59], v[28:31], v[168:171], v[56:59]\n\t" \
    "ds_read_b128 v[12:15], v16 offset:320\n\t" \
    "s_waitcnt lgkmcnt(3)\n\t" \
    "v_mfma_f32_16x16x32_bf16 v[48:51], v[32:35], v[176:179], v[48:51]\n\t" \
    "s_barrier\n\t" \
    "v_add_u32 v2, 0x10000, v2\n\t" \
    "ds_read_b128 v[24:27], v2 offset:0\n\t" \
    "s_add_u32 s61, s45, 0x4000\n\t" \
    "v_mfma_f32_16x16x32_bf16 v[52:55], v[32:35], v[180:183], v[52:55]\n\t" \
    "v_mul_f32 v80, v80, v88\n\t" \
    "v_mul_f32 v81, v81, v89\n\t" \
    "v_mfma_f32_16x16x32_bf16 v[60:63], v[36:39], v[180:183], v[60:63]\n\t" \
    "ds_read_b128 v[28:31], v2 offset:1024\n\t" \
    "v_mul_f32 v82, v82, v90\n\t" \
    "v_mfma_f32_16x16x32_bf16 v[56:59], v[36:39], v[176:179], v[56:59]\n\t" \
    "v_mul_f32 v83, v83, v91\n\t" \
    "s_waitcnt lgkmcnt(3)\n\t" \
    "v_mfma_f32_16x16x32_bf16 v[48:51], v[40:43], v[184:187], v[48:51]\n\t" \
    "ds_read_b128 v[32:35], v2 offset:2048\n\t" \
    "v_mul_f32 v84, v84, v92\n\t" \
    "v_mfma_f32_16x16x32_bf16 v[52:55], v[40:43], v[188:191], v[52:55]\n\t" \
    "v_mul_f32 v85, v85, v93\n\t" \
    "v_mul_f32 v86, v86, v94\n\t" \
    "v_mfma_f32_16x16x32_bf16 v[60:63], v[44:47], v[188:191], v[60:63]\n\t" \
    "ds_read_b128 v[36:39], v2 offset:3072\n\t" \
    "v_mul_f32 v87, v87, v95\n\t" \
    "v_mfma_f32_16x16x32_bf16 v[56:59], v[44:47], v[184:187], v[56:59]\n\t" \
    "v_mul_f32 v80, v80, v5\n\t" \
    "s_waitcnt lgkmcnt(2)\n\t" \
    "v_mfma_f32_16x16x32_bf16 v[48:51], v[24:27], v[192:195], v[48:51]\n\t" \
    "ds_read_b128 v[40:43], v2 offset:4096\n\t" \
    "v_mul_f32 v81, v81, v5\n\t" \
    "v_mfma_f32_16x16x32_bf16 v[52:55], v[24:27], v[196:199], v[52:55]\n\t" \
    "v_mul_f32 v82, v82, v5\n\t" \
    "v_mfma_f32_16x16x32_bf16 v[60:63], v[28:31], v[196:199], v[60:63]\n\t" \
    "ds_read_b128 v[44:47], v2 offset:5120\n\t" \
    "v_mul_f32 v83, v83, v5\n\t" \
    "v_mfma_f32_16x16x32_bf16 v[56:59], v[28:31], v[192:195], v[56:59]\n\t" \
    "v_mul_f32 v84, v84, v7\n\t" \
    "s_waitcnt lgkmcnt(2)\n\t" \
    "v_mfma_f32_16x16x32_bf16 v[48:51], v[32:35], v[200:203], v[48:51]\n\t" \
    "ds_read_b128 v[24:27], v2 offset:6144\n\t" \
    "v_mul_f32 v85, v85, v7\n\t" \
    "v_mfma_f32_16x16x32_bf16 v[52:55], v[32:35], v[204:207], v[52:55]\n\t" \
    "v_mul_f32 v86, v86, v7\n\t" \
    "v_mul_f32 v87, v87, v7\n\t" \
    "v_mfma_f32_16x16x32_bf16 v[60:63], v[36:39], v[204:207], v[60:63]\n\t" \
    "ds_read_b128 v[28:31], v2 offset:7168\n\t" \
    "v_cvt_pk_bf16_f32 v88, v80, v81\n\t" \
    "v_mfma_f32_16x16x32_bf16 v[56:59], v[36:39], v[200:203], v[56:59]\n\t" \
    "v_cvt_pk_bf16_f32 v89, v82, v83\n\t" \
    "s_waitcnt lgkmcnt(2)\n\t" \
    "v_mfma_f32_16x16x32_bf16 v[48:51], v[40:43], v[208:211], v[48:51]\n\t" \
    "ds_read_b128 v[32:35], v2 offset:8192\n\t" \
    "v_cvt_pk_bf16_f32 v90, v84, v85\n\t" \
    "v_mfma_f32_16x16x32_bf16 v[52:55], v[40:43], v[212:215], v[52:55]\n\t" \
    "v_cvt_pk_bf16_f32 v91, v86, v87\n\t" \
    "v_mfma_f32_16x16x32_bf16 v[60:63], v[44:47], v[212:215], v[60:63]\n\t" \
    "ds_read_b128 v[36:39], v2 offset:9216\n\t" \
    "v_permlane16_swap_b32 v88, v90\n\t" \
    "v_mfma_f32_16x16x32_bf16 v[56:59], v[44:47], v[208:211], v[56:59]\n\t" \
    "v_permlane16_swap_b32 v89, v91\n\t" \
    "s_waitcnt lgkmcnt(2)\n\t" \
    "v_mfma_f32_16x16x32_bf16 v[48:51], v[24:27], v[216:219], v[48:51]\n\t" \
    "ds_read_b128 v[40:43], v2 offset:10240\n\t" \
    "buffer_store_dwordx4 v[88:91], v1, s[36:39], s61 offen sc1\n\t" \
    "v_mfma_f32_16x16x32_bf16 v[52:55], v[24:27], v[220:223], v[52:55]\n\t" \
    "v_fmac_f32 v96, v4, v8\n\t" \
    "v_fmac_f32 v97, v4, v9\n\t" \
    "v_mfma_f32_16x16x32_bf16 v[60:63], v[28:31], v[220:223], v[60:63]\n\t" \
    "ds_read_b128 v[44:47], v2 offset:11264\n\t" \
    "v_fmac_f32 v98, v4, v10\n\t" \
    "v_mfma_f32_16x16x32_bf16 v[56:59], v[28:31], v[216:219], v[56:59]\n\t" \
    "v_fmac_f32 v99, v4, v11\n\t" \
    "s_waitcnt lgkmcnt(2)\n\t" \
    "v_mfma_f32_16x16x32_bf16 v[48:51], v[32:35], v[224:227], v[48:51]\n\t" \
    "ds_read_b128 v[24:27], v2 offset:12288\n\t" \
    "v_fmac_f32 v100, v6, v8\n\t" \
    "v_mfma_f32_16x16x32_bf16 v[52:55], v[32:35], v[228:231], v[52:55]\n\t" \
    "v_fmac_f32 v101, v6, v9\n\t" \
    "v_fmac_f32 v102, v6, v10\n\t" \
    "v_mfma_f32_16x16x32_bf16 v[60:63], v[36:39], v[228:231], v[60:63]\n\t" \
    "ds_read_b128 v[28:31], v2 offset:13312\n\t" \
    "v_fmac_f32 v103, v6, v11\n\t" \
    "v_mfma_f32_16x16x32_bf16 v[56:59], v[36:39], v[224:227], v[56:59]\n\t" \
    "ds_read_b128 v[8:11], v16 offset:384\n\t" \
    "s_waitcnt lgkmcnt(3)\n\t" \
    "v_mfma_f32_16x16x32_bf16 v[48:51], v[40:43], v[232:235], v[48:51]\n\t" \
    "ds_read_b128 v[32:35], v2 offset:14336\n\t" \
    "v_fmac_f32 v104, v4, v12\n\t" \
    "v_mfma_f32_16x16x32_bf16 v[52:55], v[40:43], v[236:239], v[52:55]\n\t" \
    "v_fmac_f32 v105, v4, v13\n\t" \
    "v_mfma_f32_16x16x32_bf16 v[60:63], v[44:47], v[236:239], v[60:63]\n\t" \
    "ds_read_b128 v[36:39], v2 offset:15360\n\t" \
    "v_fmac_f32 v106, v4, v14\n\t" \
    "v_mfma_f32_16x16x32_bf16 v[56:59], v[44:47], v[232:235], v[56:59]\n\t" \
    "v_fmac_f32 v107, v4, v15\n\t" \
    "s_waitcnt lgkmcnt(3)\n\t" \
    "v_mfma_f32_16x16x32_bf16 v[48:51], v[24:27], v[240:243], v[48:51]\n\t" \
    "s_barrier\n\t" \
    "v_add_u32 v2, 0xfffe0000, v2\n\t" \
    "ds_read_b128 v[40:43], v2 offset:0\n\t" \
    "v_fmac_f32 v108, v6, v12\n\t" \
    "v_mfma_f32_16x16x32_bf16 v[52:55], v[24:27], v[244:247], v[52:55]\n\t" \
    "v_fmac_f32 v109, v6, v13\n\t" \
    "v_fmac_f32 v110, v6, v14\n\t" \
    "v_mfma_f32_16x16x32_bf16 v[60:63], v[28:31], v[244:247], v[60:63]\n\t" \
    "ds_read_b128 v[44:47], v2 offset:1024\n\t" \
    "v_fmac_f32 v111, v6, v15\n\t" \
    "v_mfma_f32_16x16x32_bf16 v[56:59], v[28:31], v[240:243], v[56:59]\n\t" \
    "ds_read_b128 v[12:15], v16 offset:448\n\t" \
    "s_waitcnt lgkmcnt(3)\n\t" \
    "v_mfma_f32_16x16x32_bf16 v[48:51], v[32:35], v[248:251], v[48:51]\n\t" \
    "ds_read_b128 v[24:27], v2 offset:2048\n\t" \
    "s_add_u32 s61, s45, 0x8000\n\t" \
    "v_mfma_f32_16x16x32_bf16 v[52:55], v[32:35], v[252:255], v[52:55]\n\t" \
    "v_mul_f32 v96, v96, v104\n\t" \
    "v_mfma_f32_16x16x32_bf16 v[60:63], v[36:39], v[252:255], v[60:63]\n\t" \
    "ds_read_b128 v[28:31], v2 offset:3072\n\t" \
    "v_mul_f32 v97, v97, v105\n\t" \
    "v_mfma_f32_16x16x32_bf16 v[56:59], v[36:39], v[248:251], v[56:59]\n\t" \
    "v_mul_f32 v98, v98, v106\n\t" \
    "s_waitcnt lgkmcnt(3)\n\t" \
    "v_mfma_f32_16x16x32_bf16 v[64:67], v[40:43], v[128:131], 0\n\t" \
    "ds_read_b128 v[32:35], v2 offset:4096\n\t" \
    "v_mul_f32 v99, v99, v107\n\t" \
    "v_mfma_f32_16x16x32_bf16 v[68:71], v[40:43], v[132:135], 0\n\t" \
    "v_mul_f32 v100, v100, v108\n\t" \
    "v_mul_f32 v101, v101, v109\n\t" \
    "v_mfma_f32_16x16x32_bf16 v[76:79], v[44:47], v[132:135], 0\n\t" \
    "ds_read_b128 v[36:39], v2 offset:5120\n\t" \
    "v_mul_f32 v102, v102, v110\n\t" \
    "v_mfma_f32_16x16x32_bf16 v[72:75], v[44:47], v[128:131], 0\n\t" \
    "v_mul_f32 v103, v103, v111\n\t" \
    "s_waitcnt lgkmcnt(2)\n\t" \
    "v_mfma_f32_16x16x32_bf16 v[64:67], v[24:27], v[136:139], v[64:67]\n\t" \
    "ds_read_b128 v[40:43], v2 offset:6144\n\t" \
    "v_mul_f32 v96, v96, v5\n\t" \
    "v_mfma_f32_16x16x32_bf16 v[68:71], v[24:27], v[140:143], v[68:71]\n\t" \
    "v_mul_f32 v97, v97, v5\n\t" \
    "v_mul_f32 v98, v98, v5\n\t" \
    "v_mfma_f32_16x16x32_bf16 v[76:79], v[28:31], v[140:143], v[76:79]\n\t" \
    "ds_read_b128 v[44:47], v2 offset:7168\n\t" \
    "v_mul_f32 v99, v99, v5\n\t" \
    "v_mfma_f32_16x16x32_bf16 v[72:75], v[28:31], v[136:139], v[72:75]\n\t" \
    "v_mul_f32 v100, v100, v7\n\t" \
    "s_waitcnt lgkmcnt(2)\n\t" \
    "v_mfma_f32_16x16x32_bf16 v[64:67], v[32:35], v[144:147], v[64:67]\n\t" \
    "ds_read_b128 v[24:27], v2 offset:8192\n\t" \
    "v_mul_f32 v101, v101, v7\n\t" \
    "v_mfma_f32_16x16x32_bf16 v[68:71], v[32:35], v[148:151], v[68:71]\n\t" \
    "v_mul_f32 v102, v102, v7\n\t" \
    "v_mfma_f32_16x16x32_bf16 v[76:79], v[36:39], v[148:151], v[76:79]\n\t" \
    "ds_read_b128 v[28:31], v2 offset:9216\n\t" \
    "v_mul_f32 v103, v103, v7\n\t" \
    "v_mfma_f32_16x16x32_bf16 v[72:75], v[36:39], v[144:147], v[72:75]\n\t" \
    "v_cvt_pk_bf16_f32 v104, v96, v97\n\t" \
    "s_waitcnt lgkmcnt(2)\n\t" \
    "v_mfma_f32_16x16x32_bf16 v[64:67], v[40:43], v[152:155], v[64:67]\n\t" \
    "ds_read_b128 v[32:35], v2 offset:10240\n\t" \
    "v_cvt_pk_bf16_f32 v105, v98, v99\n\t" \
    "v_mfma_f32_16x16x32_bf16 v[68:71], v[40:43], v[156:159], v[68:71]\n\t" \
    "v_cvt_pk_bf16_f32 v106, v100, v101\n\t" \
    "v_cvt_pk_bf16_f32 v107, v102, v103\n\t" \
    "v_mfma_f32_16x16x32_bf16 v[76:79], v[44:47], v[156:159], v[76:79]\n\t" \
    "ds_read_b128 v[36:39], v2 offset:11264\n\t" \
    "v_permlane16_swap_b32 v104, v106\n\t" \
    "v_mfma_f32_16x16x32_bf16 v[72:75], v[44:47], v[152:155], v[72:75]\n\t" \
    "v_permlane16_swap_b32 v105, v107\n\t" \
    "s_waitcnt lgkmcnt(2)\n\t" \
    "v_mfma_f32_16x16x32_bf16 v[64:67], v[24:27], v[160:163], v[64:67]\n\t" \
    "ds_read_b128 v[40:43], v2 offset:12288\n\t" \
    "buffer_store_dwordx4 v[104:107], v1, s[36:39], s61 offen sc1\n\t" \
    "v_mfma_f32_16x16x32_bf16 v[68:71], v[24:27], v[164:167], v[68:71]\n\t" \
    "v_fmac_f32 v112, v4, v8\n\t" \
    "v_mfma_f32_16x16x32_bf16 v[76:79], v[28:31], v[164:167], v[76:79]\n\t" \
    "ds_read_b128 v[44:47], v2 offset:13312\n\t" \
    "v_fmac_f32 v113, v4, v9\n\t" \
    "v_mfma_f32_16x16x32_bf16 v[72:75], v[28:31], v[160:163], v[72:75]\n\t" \
    "v_fmac_f32 v114, v4, v10\n\t" \
    "s_waitcnt lgkmcnt(2)\n\t" \
    "v_mfma_f32_16x16x32_bf16 v[64:67], v[32:35], v[168:171], v[64:67]\n\t" \
    "ds_read_b128 v[24:27], v2 offset:14336\n\t" \
    "v_fmac_f32 v115, v4, v11\n\t" \
    "v_mfma_f32_16x16x32_bf16 v[68:71], v[32:35], v[172:175], v[68:71]\n\t" \
    "v_fmac_f32 v116, v6, v8\n\t" \
    "v_fmac_f32 v117, v6, v9\n\t" \
    "v_mfma_f32_16x16x32_bf16 v[76:79], v[36:39], v[172:175], v[76:79]\n\t" \
    "ds_read_b128 v[28:31], v2 offset:15360\n\t" \
    "v_fmac_f32 v118, v6, v10\n\t" \
    "v_mfma_f32_16x16x32_bf16 v[72:75], v[36:39], v[168:171], v[72:75]\n\t" \
    "v_fmac_f32 v119, v6, v11\n\t" \
    "s_waitcnt lgkmcnt(2)\n\t" \
    "v_mfma_f32_16x16x32_bf16 v[64:67], v[40:43], v[176:179], v[64:67]\n\t" \
    "s_barrier\n\t" \
    "ds_read_b128 v[32:35], v2 offset:16384\n\t" \
    "ds_read_b128 v[8:11], v16 offset:512\n\t" \
    "v_mfma_f32_16x16x32_bf16 v[68:71], v[40:43], v[180:183], v[68:71]\n\t" \
    "v_fmac_f32 v120, v4, v12\n\t" \
    "v_fmac_f32 v121, v4, v13\n\t" \
    "v_mfma_f32_16x16x32_bf16 v[76:79], v[44:47], v[180:183], v[76:79]\n\t" \
    "ds_read_b128 v[36:39], v2 offset:17408\n\t" \
    "v_fmac_f32 v122, v4, v14\n\t" \
    "v_mfma_f32_16x16x32_bf16 v[72:75], v[44:47], v[176:179], v[72:75]\n\t" \
    "v_fmac_f32 v123, v4, v15\n\t" \
    "s_waitcnt lgkmcnt(3)\n\t" \
    "v_mfma_f32_16x16x32_bf16 v[64:67], v[24:27], v[184:187], v[64:67]\n\t" \
    "ds_read_b128 v[40:43], v2 offset:18432\n\t" \
    "v_fmac_f32 v124, v6, v12\n\t" \
    "v_mfma_f32_16x16x32_bf16 v[68:71], v[24:27], v[188:191], v[68:71]\n\t" \
    "v_fmac_f32 v125, v6, v13\n\t" \
    "v_mfma_f32_16x16x32_bf16 v[76:79], v[28:31], v[188:191], v[76:79]\n\t" \
    "ds_read_b128 v[44:47], v2 offset:19456\n\t" \
    "v_fmac_f32 v126, v6, v14\n\t" \
    "v_mfma_f32_16x16x32_bf16 v[72:75], v[28:31], v[184:187], v[72:75]\n\t" \
    "v_fmac_f32 v127, v6, v15\n\t" \
    "s_waitcnt lgkmcnt(2)\n\t" \
    "v_mfma_f32_16x16x32_bf16 v[64:67], v[32:35], v[192:195], v[64:67]\n\t" \
    "ds_read_b128 v[24:27], v2 offset:20480\n\t" \
    "ds_read_b128 v[12:15], v16 offset:576\n\t" \
    "v_mfma_f32_16x16x32_bf16 v[68:71], v[32:35], v[196:199], v[68:71]\n\t" \
    "s_add_u32 s61, s45, 0xc000\n\t" \
    "v_mul_f32 v112, v112, v120\n\t" \
    "v_mfma_f32_16x16x32_bf16 v[76:79], v[36:39], v[196:199], v[76:79]\n\t" \
    "ds_read_b128 v[28:31], v2 offset:21504\n\t" \
    "v_mul_f32 v113, v113, v121\n\t" \
    "v_mfma_f32_16x16x32_bf16 v[72:75], v[36:39], v[192:195], v[72:75]\n\t" \
    "v_mul_f32 v114, v114, v122\n\t" \
    "s_waitcnt lgkmcnt(3)\n\t" \
    "v_mfma_f32_16x16x32_bf16 v[64:67], v[40:43], v[200:203], v[64:67]\n\t" \
    "ds_read_b128 v[32:35], v2 offset:22528\n\t" \
    "v_mul_f32 v115, v115, v123\n\t" \
    "v_mfma_f32_16x16x32_bf16 v[68:71], v[40:43], v[204:207], v[68:71]\n\t" \
    "v_mul_f32 v116, v116, v124\n\t" \
    "v_mfma_f32_16x16x32_bf16 v[76:79], v[44:47], v[204:207], v[76:79]\n\t" \
    "ds_read_b128 v[36:39], v2 offset:23552\n\t" \
    "v_mul_f32 v117, v117, v125\n\t" \
    "v_mfma_f32_16x16x32_bf16 v[72:75], v[44:47], v[200:203], v[72:75]\n\t" \
    "v_mul_f32 v118, v118, v126\n\t" \
    "s_waitcnt lgkmcnt(2)\n\t" \
    "v_mfma_f32_16x16x32_bf16 v[64:67], v[24:27], v[208:211], v[64:67]\n\t" \
    "ds_read_b128 v[40:43], v2 offset:24576\n\t" \
    "v_mul_f32 v119, v119, v127\n\t" \
    "v_mfma_f32_16x16x32_bf16 v[68:71], v[24:27], v[212:215], v[68:71]\n\t" \
    "v_mul_f32 v112, v112, v5\n\t" \
    "v_mul_f32 v113, v113, v5\n\t" \
    "v_mfma_f32_16x16x32_bf16 v[76:79], v[28:31], v[212:215], v[76:79]\n\t" \
    "ds_read_b128 v[44:47], v2 offset:25600\n\t" \
    "v_mul_f32 v114, v114, v5\n\t" \
    "v_mfma_f32_16x16x32_bf16 v[72:75], v[28:31], v[208:211], v[72:75]\n\t" \
    "v_mul_f32 v115, v115, v5\n\t" \
    "s_waitcnt lgkmcnt(2)\n\t" \
    "v_mfma_f32_16x16x32_bf16 v[64:67], v[32:35], v[216:219], v[64:67]\n\t" \
    "ds_read_b128 v[24:27], v2 offset:26624\n\t" \
    "v_mul_f32 v116, v116, v7\n\t" \
    "v_mfma_f32_16x16x32_bf16 v[68:71], v[32:35], v[220:223], v[68:71]\n\t" \
    "v_mul_f32 v117, v117, v7\n\t" \
    "v_mul_f32 v118, v118, v7\n\t" \
    "v_mfma_f32_16x16x32_bf16 v[76:79], v[36:39], v[220:223], v[76:79]\n\t" \
    "ds_read_b128 v[28:31], v2 offset:27648\n\t" \
    "v_mul_f32 v119, v119, v7\n\t" \
    "v_mfma_f32_16x16x32_bf16 v[72:75], v[36:39], v[216:219], v[72:75]\n\t" \
    "v_cvt_pk_bf16_f32 v120, v112, v113\n\t" \
    "s_waitcnt lgkmcnt(2)\n\t" \
    "v_mfma_f32_16x16x32_bf16 v[64:67], v[40:43], v[224:227], v[64:67]\n\t" \
    "ds_read_b128 v[32:35], v2 offset:28672\n\t" \
    "v_cvt_pk_bf16_f32 v121, v114, v115\n\t" \
    "v_mfma_f32_16x16x32_bf16 v[68:71], v[40:43], v[228:231], v[68:71]\n\t" \
    "v_cvt_pk_bf16_f32 v122, v116, v117\n\t" \
    "v_mfma_f32_16x16x32_bf16 v[76:79], v[44:47], v[228:231], v[76:79]\n\t" \
    "ds_read_b128 v[36:39], v2 offset:29696\n\t" \
    "v_cvt_pk_bf16_f32 v123, v118, v119\n\t" \
    "v_mfma_f32_16x16x32_bf16 v[72:75], v[44:47], v[224:227], v[72:75]\n\t" \
    "v_permlane16_swap_b32 v120, v122\n\t" \
    "s_waitcnt lgkmcnt(2)\n\t" \
    "v_mfma_f32_16x16x32_bf16 v[64:67], v[24:27], v[232:235], v[64:67]\n\t" \
    "ds_read_b128 v[40:43], v2 offset:30720\n\t" \
    "v_permlane16_swap_b32 v121, v123\n\t" \
    "v_mfma_f32_16x16x32_bf16 v[68:71], v[24:27], v[236:239], v[68:71]\n\t" \
    "buffer_store_dwordx4 v[120:123], v1, s[36:39], s61 offen sc1\n\t" \
    "v_fmac_f32 v48, v4, v8\n\t" \
    "v_mfma_f32_16x16x32_bf16 v[76:79], v[28:31], v[236:239], v[76:79]\n\t" \
    "ds_read_b128 v[44:47], v2 offset:31744\n\t" \
    "v_fmac_f32 v49, v4, v9\n\t" \
    "v_mfma_f32_16x16x32_bf16 v[72:75], v[28:31], v[232:235], v[72:75]\n\t" \
    "v_fmac_f32 v50, v4, v10\n\t" \
    "s_waitcnt lgkmcnt(2)\n\t" \
    "v_mfma_f32_16x16x32_bf16 v[64:67], v[32:35], v[240:243], v[64:67]\n\t" \
    "s_barrier\n\t" \
    "ds_read_b128 v[24:27], v2 offset:32768\n\t" \
    "v_fmac_f32 v51, v4, v11\n\t" \
    "v_mfma_f32_16x16x32_bf16 v[68:71], v[32:35], v[244:247], v[68:71]\n\t" \
    "v_fmac_f32 v52, v6, v8\n\t" \
    "v_mfma_f32_16x16x32_bf16 v[76:79], v[36:39], v[244:247], v[76:79]\n\t" \
    "ds_read_b128 v[28:31], v2 offset:33792\n\t" \
    "v_fmac_f32 v53, v6, v9\n\t" \
    "v_mfma_f32_16x16x32_bf16 v[72:75], v[36:39], v[240:243], v[72:75]\n\t" \
    "v_fmac_f32 v54, v6, v10\n\t" \
    "s_waitcnt lgkmcnt(2)\n\t" \
    "v_mfma_f32_16x16x32_bf16 v[64:67], v[40:43], v[248:251], v[64:67]\n\t" \
    "ds_read_b128 v[32:35], v2 offset:34816\n\t" \
    "v_fmac_f32 v55, v6, v11\n\t" \
    "v_mfma_f32_16x16x32_bf16 v[68:71], v[40:43], v[252:255], v[68:71]\n\t" \
    "ds_read_b128 v[8:11], v16 offset:640\n\t" \
    "v_fmac_f32 v56, v4, v12\n\t" \
    "v_mfma_f32_16x16x32_bf16 v[76:79], v[44:47], v[252:255], v[76:79]\n\t" \
    "ds_read_b128 v[36:39], v2 offset:35840\n\t" \
    "v_fmac_f32 v57, v4, v13\n\t" \
    "v_mfma_f32_16x16x32_bf16 v[72:75], v[44:47], v[248:251], v[72:75]\n\t" \
    "v_fmac_f32 v58, v4, v14\n\t" \
    "s_waitcnt lgkmcnt(3)\n\t" \
    "v_mfma_f32_16x16x32_bf16 v[80:83], v[24:27], v[128:131], 0\n\t" \
    "ds_read_b128 v[40:43], v2 offset:36864\n\t" \
    "v_fmac_f32 v59, v4, v15\n\t" \
    "v_mfma_f32_16x16x32_bf16 v[84:87], v[24:27], v[132:135], 0\n\t" \
    "v_fmac_f32 v60, v6, v12\n\t" \
    "v_fmac_f32 v61, v6, v13\n\t" \
    "v_mfma_f32_16x16x32_bf16 v[92:95], v[28:31], v[132:135], 0\n\t" \
    "ds_read_b128 v[44:47], v2 offset:37888\n\t" \
    "v_fmac_f32 v62, v6, v14\n\t" \
    "v_mfma_f32_16x16x32_bf16 v[88:91], v[28:31], v[128:131], 0\n\t" \
    "v_fmac_f32 v63, v6, v15\n\t" \
    "s_waitcnt lgkmcnt(2)\n\t" \
    "v_mfma_f32_16x16x32_bf16 v[80:83], v[32:35], v[136:139], v[80:83]\n\t" \
    "ds_read_b128 v[24:27], v2 offset:38912\n\t" \
    "ds_read_b128 v[12:15], v16 offset:704\n\t" \
    "v_mfma_f32_16x16x32_bf16 v[84:87], v[32:35], v[140:143], v[84:87]\n\t" \
    "s_add_u32 s61, s45, 0x10000\n\t" \
    "v_mfma_f32_16x16x32_bf16 v[92:95], v[36:39], v[140:143], v[92:95]\n\t" \
    "ds_read_b128 v[28:31], v2 offset:39936\n\t" \
    "v_mul_f32 v48, v48, v56\n\t" \
    "v_mfma_f32_16x16x32_bf16 v[88:91], v[36:39], v[136:139], v[88:91]\n\t" \
    "v_mul_f32 v49, v49, v57\n\t" \
    "s_mov_b32 s60, s46\n\t" \
    "buffer_load_dwordx4 v[128:131], v1, s[36:39], s60 offen sc1\n\t" \
    "s_waitcnt lgkmcnt(3)\n\t" \
    "v_mfma_f32_16x16x32_bf16 v[80:83], v[40:43], v[144:147], v[80:83]\n\t" \
    "ds_read_b128 v[32:35], v2 offset:40960\n\t" \
    "v_mul_f32 v50, v50, v58\n\t" \
    "v_mfma_f32_16x16x32_bf16 v[84:87], v[40:43], v[148:151], v[84:87]\n\t" \
    "v_mul_f32 v51, v51, v59\n\t" \
    "v_mul_f32 v52, v52, v60\n\t" \
    "v_mfma_f32_16x16x32_bf16 v[92:95], v[44:47], v[148:151], v[92:95]\n\t" \
    "ds_read_b128 v[36:39], v2 offset:41984\n\t" \
    "v_mul_f32 v53, v53, v61\n\t" \
    "v_mfma_f32_16x16x32_bf16 v[88:91], v[44:47], v[144:147], v[88:91]\n\t" \
    "v_mul_f32 v54, v54, v62\n\t" \
    "s_waitcnt lgkmcnt(2)\n\t" \
    "v_mfma_f32_16x16x32_bf16 v[80:83], v[24:27], v[152:155], v[80:83]\n\t" \
    "ds_read_b128 v[40:43], v2 offset:43008\n\t" \
    "v_mul_f32 v55, v55, v63\n\t" \
    "v_mfma_f32_16x16x32_bf16 v[84:87], v[24:27], v[156:159], v[84:87]\n\t" \
    "v_mul_f32 v48, v48, v5\n\t" \
    "v_mfma_f32_16x16x32_bf16 v[92:95], v[28:31], v[156:159], v[92:95]\n\t" \
    "ds_read_b128 v[44:47], v2 offset:44032\n\t" \
    "v_mul_f32 v49, v49, v5\n\t" \
    "v_mfma_f32_16x16x32_bf16 v[88:91], v[28:31], v[152:155], v[88:91]\n\t" \
    "v_mul_f32 v50, v50, v5\n\t" \
    "buffer_load_dwordx4 v[132:135], v1, s[36:39], s60 offen offset:2048 sc1\n\t" \
    "s_waitcnt lgkmcnt(2)\n\t" \
    "v_mfma_f32_16x16x32_bf16 v[80:83], v[32:35], v[160:163], v[80:83]\n\t" \
    "ds_read_b128 v[24:27], v2 offset:45056\n\t" \
    "v_mul_f32 v51, v51, v5\n\t" \
    "v_mfma_f32_16x16x32_bf16 v[84:87], v[32:35], v[164:167], v[84:87]\n\t" \
    "v_mul_f32 v52, v52, v7\n\t" \
    "v_mul_f32 v53, v53, v7\n\t" \
    "v_mfma_f32_16x16x32_bf16 v[92:95], v[36:39], v[164:167], v[92:95]\n\t" \
    "ds_read_b128 v[28:31], v2 offset:46080\n\t" \
    "v_mul_f32 v54, v54, v7\n\t" \
    "v_mfma_f32_16x16x32_bf16 v[88:91], v[36:39], v[160:163], v[88:91]\n\t" \
    "v_mul_f32 v55, v55, v7\n\t" \
    "s_waitcnt lgkmcnt(2)\n\t" \
    "v_mfma_f32_16x16x32_bf16 v[80:83], v[40:43], v[168:171], v[80:83]\n\t" \
    "ds_read_b128 v[32:35], v2 offset:47104\n\t" \
    "v_cvt_pk_bf16_f32 v56, v48, v49\n\t" \
    "v_mfma_f32_16x16x32_bf16 v[84:87], v[40:43], v[172:175], v[84:87]\n\t" \
    "v_cvt_pk_bf16_f32 v57, v50, v51\n\t" \
    "v_cvt_pk_bf16_f32 v58, v52, v53\n\t" \
    "v_mfma_f32_16x16x32_bf16 v[92:95], v[44:47], v[172:175], v[92:95]\n\t" \
    "ds_read_b128 v[36:39], v2 offset:48128\n\t" \
    "v_cvt_pk_bf16_f32 v59, v54, v55\n\t" \
    "v_mfma_f32_16x16x32_bf16 v[88:91], v[44:47], v[168:171], v[88:91]\n\t" \
    "v_permlane16_swap_b32 v56, v58\n\t" \
    "s_add_u32 s60, s60, 0x1000\n\t" \
    "buffer_load_dwordx4 v[136:139], v1, s[36:39], s60 offen sc1\n\t" \
    "s_waitcnt lgkmcnt(2)\n\t" \
    "v_mfma_f32_16x16x32_bf16 v[80:83], v[24:27], v[176:179], v[80:83]\n\t" \
    "s_barrier\n\t" \
    "ds_read_b128 v[40:43], v2 offset:49152\n\t" \
    "v_permlane16_swap_b32 v57, v59\n\t" \
    "v_mfma_f32_16x16x32_bf16 v[84:87], v[24:27], v[180:183], v[84:87]\n\t" \
    "buffer_store_dwordx4 v[56:59], v1, s[36:39], s61 offen sc1\n\t" \
    "v_mfma_f32_16x16x32_bf16 v[92:95], v[28:31], v[180:183], v[92:95]\n\t" \
    "ds_read_b128 v[44:47], v2 offset:50176\n\t" \
    "v_and_b32 v18, 15, v0\n\t" \
    "v_mfma_f32_16x16x32_bf16 v[88:91], v[28:31], v[176:179], v[88:91]\n\t" \
    "v_bfe_u32 v17, v0, 4, 1\n\t" \
    "s_waitcnt lgkmcnt(2)\n\t" \
    "v_mfma_f32_16x16x32_bf16 v[80:83], v[32:35], v[184:187], v[80:83]\n\t" \
    "ds_read_b128 v[24:27], v2 offset:51200\n\t" \
    "v_lshl_add_u32 v18, v18, 1, v17\n\t" \
    "v_mfma_f32_16x16x32_bf16 v[84:87], v[32:35], v[188:191], v[84:87]\n\t" \
    "v_lshlrev_b32 v18, 2, v18\n\t" \
    "v_lshrrev_b32 v17, 5, v0\n\t" \
    "v_mfma_f32_16x16x32_bf16 v[92:95], v[36:39], v[188:191], v[92:95]\n\t" \
    "ds_read_b128 v[28:31], v2 offset:52224\n\t" \
    "v_lshl_add_u32 v18, v17, 9, v18\n\t" \
    "v_mfma_f32_16x16x32_bf16 v[88:91], v[36:39], v[184:187], v[88:91]\n\t" \
    "v_add_u32 v18, s47, v18\n\t" \
    "buffer_load_dwordx4 v[140:143], v1, s[36:39], s60 offen offset:2048 sc1\n\t" \
    "s_waitcnt lgkmcnt(2)\n\t" \
    "v_mfma_f32_16x16x32_bf16 v[80:83], v[40:43], v[192:195], v[80:83]\n\t" \
    "ds_read_b128 v[32:35], v2 offset:53248\n\t" \
    "ds_read2st64_b32 v[112:113], v18 offset0:0 offset1:1\n\t" \
    "v_mfma_f32_16x16x32_bf16 v[84:87], v[40:43], v[196:199], v[84:87]\n\t" \
    "ds_read2st64_b32 v[114:115], v18 offset0:4 offset1:5\n\t" \
    "v_mfma_f32_16x16x32_bf16 v[92:95], v[44:47], v[196:199], v[92:95]\n\t" \
    "ds_read_b128 v[36:39], v2 offset:54272\n\t" \
    "ds_read2st64_b32 v[116:117], v18 offset0:8 offset1:9\n\t" \
    "v_mfma_f32_16x16x32_bf16 v[88:91], v[44:47], v[192:195], v[88:91]\n\t" \
    "ds_read2st64_b32 v[118:119], v18 offset0:12 offset1:13\n\t" \
    "s_waitcnt lgkmcnt(6)\n\t" \
    "v_mfma_f32_16x16x32_bf16 v[80:83], v[24:27], v[200:203], v[80:83]\n\t" \
    "ds_read_b128 v[40:43], v2 offset:55296\n\t" \
    "v_and_b32 v3, 15, v0\n\t" \
    "v_mfma_f32_16x16x32_bf16 v[84:87], v[24:27], v[204:207], v[84:87]\n\t" \
    "v_bfe_u32 v17, v0, 4, 1\n\t" \
    "v_lshl_add_u32 v3, v3, 1, v17\n\t" \
    "v_mfma_f32_16x16x32_bf16 v[92:95], v[28:31], v[204:207], v[92:95]\n\t" \
    "ds_read_b128 v[44:47], v2 offset:56320\n\t" \
    "v_lshlrev_b32 v3, 10, v3\n\t" \
    "v_mfma_f32_16x16x32_bf16 v[88:91], v[28:31], v[200:203], v[88:91]\n\t" \
    "v_lshrrev_b32 v18, 5, v0\n\t" \
    "s_add_u32 s60, s60, 0x1000\n\t" \
    "buffer_load_dwordx4 v[144:147], v1, s[36:39], s60 offen sc1\n\t" \
    "s_waitcnt lgkmcnt(4)\n\t" \
    "v_mfma_f32_16x16x32_bf16 v[80:83], v[32:35], v[208:211], v[80:83]\n\t" \
    "ds_read_b128 v[24:27], v2 offset:57344\n\t" \
    "v_lshl_add_u32 v3, v18, 4, v3\n\t" \
    "v_mfma_f32_16x16x32_bf16 v[84:87], v[32:35], v[212:215], v[84:87]\n\t" \
    "s_lshl_b32 s62, %1, 12\n\t" \
    "s_add_u32 s62, s62, 0\n\t" \
    "v_mfma_f32_16x16x32_bf16 v[92:95], v[36:39], v[212:215], v[92:95]\n\t" \
    "ds_read_b128 v[28:31], v2 offset:58368\n\t" \
    "v_add_u32 v19, s62, v1\n\t" \
    "v_mfma_f32_16x16x32_bf16 v[88:91], v[36:39], v[208:211], v[88:91]\n\t" \
    "v_xor_b32 v22, 0x1000, v19\n\t" \
    "s_waitcnt lgkmcnt(2)\n\t" \
    "v_mfma_f32_16x16x32_bf16 v[80:83], v[40:43], v[216:219], v[80:83]\n\t" \
    "ds_read_b128 v[32:35], v2 offset:59392\n\t" \
    "v_lshlrev_b32 v96, 16, v112\n\t" \
    "v_mfma_f32_16x16x32_bf16 v[84:87], v[40:43], v[220:223], v[84:87]\n\t" \
    "v_and_b32 v97, 0xffff0000, v112\n\t" \
    "v_mfma_f32_16x16x32_bf16 v[92:95], v[44:47], v[220:223], v[92:95]\n\t" \
    "ds_read_b128 v[36:39], v2 offset:60416\n\t" \
    "v_lshlrev_b32 v98, 16, v113\n\t" \
    "v_mfma_f32_16x16x32_bf16 v[88:91], v[44:47], v[216:219], v[88:91]\n\t" \
    "v_and_b32 v99, 0xffff0000, v113\n\t" \
    "buffer_load_dwordx4 v[148:151], v1, s[36:39], s60 offen offset:2048 sc1\n\t" \
    "s_waitcnt lgkmcnt(2)\n\t" \
    "v_mfma_f32_16x16x32_bf16 v[80:83], v[24:27], v[224:227], v[80:83]\n\t" \
    "ds_read_b128 v[40:43], v2 offset:61440\n\t" \
    "v_lshlrev_b32 v100, 16, v114\n\t" \
    "v_mfma_f32_16x16x32_bf16 v[84:87], v[24:27], v[228:231], v[84:87]\n\t" \
    "v_and_b32 v101, 0xffff0000, v114\n\t" \
    "v_lshlrev_b32 v102, 16, v115\n\t" \
    "v_mfma_f32_16x16x32_bf16 v[92:95], v[28:31], v[228:231], v[92:95]\n\t" \
    "ds_read_b128 v[44:47], v2 offset:62464\n\t" \
    "v_and_b32 v103, 0xffff0000, v115\n\t" \
    "v_mfma_f32_16x16x32_bf16 v[88:91], v[28:31], v[224:227], v[88:91]\n\t" \
    "v_lshlrev_b32 v104, 16, v116\n\t" \
    "s_waitcnt lgkmcnt(2)\n\t" \
    "v_mfma_f32_16x16x32_bf16 v[80:83], v[32:35], v[232:235], v[80:83]\n\t" \
    "ds_read_b128 v[24:27], v2 offset:63488\n\t" \
    "v_and_b32 v105, 0xffff0000, v116\n\t" \
    "v_mfma_f32_16x16x32_bf16 v[84:87], v[32:35], v[236:239], v[84:87]\n\t" \
    "v_lshlrev_b32 v106, 16, v117\n\t" \
    "v_mfma_f32_16x16x32_bf16 v[92:95], v[36:39], v[236:239], v[92:95]\n\t" \
    "ds_read_b128 v[28:31], v2 offset:64512\n\t" \
    "v_and_b32 v107, 0xffff0000, v117\n\t" \
    "v_mfma_f32_16x16x32_bf16 v[88:91], v[36:39], v[232:235], v[88:91]\n\t" \
    "v_lshlrev_b32 v108, 16, v118\n\t" \
    "s_add_u32 s60, s60, 0x1000\n\t" \
    "buffer_load_dwordx4 v[152:155], v1, s[36:39], s60 offen sc1\n\t" \
    "s_waitcnt lgkmcnt(2)\n\t" \
    "v_mfma_f32_16x16x32_bf16 v[80:83], v[40:43], v[240:243], v[80:83]\n\t" \
    "s_barrier\n\t" \
    "v_add_u32 v20, 0x10000, v20\n\t" \
    "v_add_u32 v21, 0x10000, v21\n\t" \
    "ds_read_b64_tr_b16 v[192:193], v20 offset:0\n\t" \
    "ds_read_b64_tr_b16 v[194:195], v20 offset:512\n\t" \
    "v_and_b32 v109, 0xffff0000, v118\n\t" \
    "v_mfma_f32_16x16x32_bf16 v[84:87], v[40:43], v[244:247], v[84:87]\n\t" \
    "v_lshlrev_b32 v110, 16, v119\n\t" \
    "v_and_b32 v111, 0xffff0000, v119\n\t" \
    "v_mfma_f32_16x16x32_bf16 v[92:95], v[44:47], v[244:247], v[92:95]\n\t" \
    "ds_read_b64_tr_b16 v[196:197], v21 offset:0\n\t" \
    "ds_read_b64_tr_b16 v[198:199], v21 offset:512\n\t" \
    "v_mfma_f32_16x16x32_bf16 v[88:91], v[44:47], v[240:243], v[88:91]\n\t" \
    "s_waitcnt lgkmcnt(4)\n\t" \
    "v_mfma_f32_16x16x32_bf16 v[80:83], v[24:27], v[248:251], v[80:83]\n\t" \
    "ds_read_b64_tr_b16 v[200:201], v20 offset:4096\n\t" \
    "ds_read_b64_tr_b16 v[202:203], v20 offset:4608\n\t" \
    "v_mfma_f32_16x16x32_bf16 v[84:87], v[24:27], v[252:255], v[84:87]\n\t" \
    "v_mfma_f32_16x16x32_bf16 v[92:95], v[28:31], v[252:255], v[92:95]\n\t" \
    "ds_read_b64_tr_b16 v[204:205], v21 offset:4096\n\t" \
    "ds_read_b64_tr_b16 v[206:207], v21 offset:4608\n\t" \
    "v_mfma_f32_16x16x32_bf16 v[88:91], v[28:31], v[248:251], v[88:91]\n\t" \
    "buffer_load_dwordx4 v[156:159], v1, s[36:39], s60 offen offset:2048 sc1\n\t" \
    "ds_read_b64_tr_b16 v[208:209], v20 offset:8192\n\t" \
    "ds_read_b64_tr_b16 v[210:211], v20 offset:8704\n\t" \
    "ds_read_b64_tr_b16 v[212:213], v21 offset:8192\n\t" \
    "ds_read_b64_tr_b16 v[214:215], v21 offset:8704\n\t" \
    "s_add_u32 s60, s60, 0x1000\n\t" \
    "buffer_load_dwordx4 v[160:163], v1, s[36:39], s60 offen sc1\n\t" \
    "buffer_load_dwordx4 v[164:167], v1, s[36:39], s60 offen offset:2048 sc1\n\t" \
    "s_add_u32 s60, s60, 0x1000\n\t" \
    "buffer_load_dwordx4 v[168:171], v1, s[36:39], s60 offen sc1\n\t" \
    "buffer_load_dwordx4 v[172:175], v1, s[36:39], s60 offen offset:2048 sc1\n\t" \
    "s_waitcnt vmcnt(12)\n\t" \
    "v_max3_u32 v17, v128, v129, v130\n\t" \
    "v_max_u32 v17, v17, v131\n\t" \
    "v_fmac_f32 v64, v4, v8\n\t" \
    "v_fmac_f32 v65, v4, v9\n\t" \
    "v_fmac_f32 v66, v4, v10\n\t" \
    "v_fmac_f32 v67, v4, v11\n\t" \
    "s_waitcnt vmcnt(11)\n\t" \
    "v_max3_u32 v17, v132, v133, v17\n\t" \
    "v_max3_u32 v17, v134, v135, v17\n\t" \
    "v_fmac_f32 v68, v6, v8\n\t" \
    "v_fmac_f32 v69, v6, v9\n\t" \
    "v_fmac_f32 v70, v6, v10\n\t" \
    "v_fmac_f32 v71, v6, v11\n\t" \
    "s_waitcnt vmcnt(10)\n\t" \
    "v_max3_u32 v17, v136, v137, v17\n\t" \
    "v_max3_u32 v17, v138, v139, v17\n\t" \
    "s_waitcnt vmcnt(8)\n\t" \
    "v_max3_u32 v17, v140, v141, v17\n\t" \
    "v_max3_u32 v17, v142, v143, v17\n\t" \
    "v_cmp_eq_u32 vcc, -1, v17\n\t" \
    "s_and_b64 vcc, exec, vcc\n\t" \
    "s_cbranch_vccnz .Lre0_%=\n\t" \
    ".Lok0_%=:\n\t" \
    "s_waitcnt lgkmcnt(8)\n\t" \
    "v_mfma_f32_32x32x16_bf16 v[96:111], v[192:195], v[128:131], v[96:111]\n\t" \
    "ds_read_b64_tr_b16 v[216:217], v20 offset:12288\n\t" \
    "ds_read_b64_tr_b16 v[218:219], v20 offset:12800\n\t" \
    "s_waitcnt vmcnt(7)\n\t" \
    "v_max3_u32 v17, v144, v145, v146\n\t" \
    "v_max_u32 v17, v17, v147\n\t" \
    "v_mfma_f32_32x32x16_bf16 v[112:127], v[196:199], v[128:131], 0\n\t" \
    "ds_read_b64_tr_b16 v[220:221], v21 offset:12288\n\t" \
    "ds_read_b64_tr_b16 v[222:223], v21 offset:12800\n\t" \
    "ds_read_b128 v[8:11], v16 offset:768\n\t" \
    "v_fmac_f32 v72, v4, v12\n\t" \
    "v_fmac_f32 v73, v4, v13\n\t" \
    "s_add_u32 s60, s60, 0x1000\n\t" \
    "buffer_load_dwordx4 v[176:179], v1, s[36:39], s60 offen sc1\n\t" \
    "s_waitcnt lgkmcnt(9)\n\t" \
    "v_mfma_f32_32x32x16_bf16 v[96:111], v[200:203], v[132:135], v[96:111]\n\t" \
    "s_barrier\n\t" \
    "ds_read_b64_tr_b16 v[192:193], v20 offset:32768\n\t" \
    "ds_read_b64_tr_b16 v[194:195], v20 offset:33280\n\t" \
    "s_waitcnt vmcnt(7)\n\t" \
    "v_max3_u32 v17, v148, v149, v17\n\t" \
    "v_max3_u32 v17, v150, v151, v17\n\t" \
    "v_fmac_f32 v74, v4, v14\n\t" \
    "v_fmac_f32 v75, v4, v15\n\t" \
    "v_fmac_f32 v76, v6, v12\n\t" \
    "v_mfma_f32_32x32x16_bf16 v[112:127], v[204:207], v[132:135], v[112:127]\n\t" \
    "ds_read_b64_tr_b16 v[196:197], v21 offset:32768\n\t" \
    "ds_read_b64_tr_b16 v[198:199], v21 offset:33280\n\t" \
    "v_fmac_f32 v77, v6, v13\n\t" \
    "v_fmac_f32 v78, v6, v14\n\t" \
    "v_fmac_f32 v79, v6, v15\n\t" \
    "buffer_load_dwordx4 v[180:183], v1, s[36:39], s60 offen offset:2048 sc1\n\t" \
    "s_waitcnt lgkmcnt(9)\n\t" \
    "v_mfma_f32_32x32x16_bf16 v[96:111], v[208:211], v[136:139], v[96:111]\n\t" \
    "ds_read_b64_tr_b16 v[200:201], v20 offset:36864\n\t" \
    "ds_read_b64_tr_b16 v[202:203], v20 offset:37376\n\t" \
    "s_waitcnt vmcnt(7)\n\t" \
    "v_max3_u32 v17, v152, v153, v17\n\t" \
    "v_max3_u32 v17, v154, v155, v17\n\t" \
    "v_mfma_f32_32x32x16_bf16 v[112:127], v[212:215], v[136:139], v[112:127]\n\t" \
    "ds_read_b64_tr_b16 v[204:205], v21 offset:36864\n\t" \
    "ds_read_b64_tr_b16 v[206:207], v21 offset:37376\n\t" \
    "ds_read_b128 v[12:15], v16 offset:832\n\t" \
    "s_add_u32 s61, s45, 0x14000\n\t" \
    "v_mul_f32 v64, v64, v72\n\t" \
    "s_add_u32 s60, s60, 0x1000\n\t" \
    "buffer_load_dwordx4 v[184:187], v1, s[36:39], s60 offen sc1\n\t" \
    "s_waitcnt lgkmcnt(10)\n\t" \
    "v_mfma_f32_32x32x16_bf16 v[96:111], v[216:219], v[140:143], v[96:111]\n\t" \
    "ds_read_b64_tr_b16 v[208:209], v20 offset:40960\n\t" \
    "ds_read_b64_tr_b16 v[210:211], v20 offset:41472\n\t" \
    "s_waitcnt vmcnt(7)\n\t" \
    "v_max3_u32 v17, v156, v157, v17\n\t" \
    "v_max3_u32 v17, v158, v159, v17\n\t" \
    "v_mul_f32 v65, v65, v73\n\t" \
    "v_mul_f32 v66, v66, v74\n\t" \
    "v_mul_f32 v67, v67, v75\n\t" \
    "v_mfma_f32_32x32x16_bf16 v[112:127], v[220:223], v[140:143], v[112:127]\n\t" \
    "ds_read_b64_tr_b16 v[212:213], v21 offset:40960\n\t" \
    "ds_read_b64_tr_b16 v[214:215], v21 offset:41472\n\t" \
    "v_mul_f32 v68, v68, v76\n\t" \
    "v_mul_f32 v69, v69, v77\n\t" \
    "v_mul_f32 v70, v70, v78\n\t" \
    "buffer_load_dwordx4 v[188:191], v1, s[36:39], s60 offen offset:2048 sc1\n\t" \
    "v_cmp_eq_u32 vcc, -1, v17\n\t" \
    "s_and_b64 vcc, exec, vcc\n\t" \
    "s_cbranch_vccnz .Lre1_%=\n\t" \
    ".Lok1_%=:\n\t" \
    "s_waitcnt lgkmcnt(9)\n\t" \
    "v_mfma_f32_32x32x16_bf16 v[96:111], v[192:195], v[144:147], v[96:111]\n\t" \
    "ds_read_b64_tr_b16 v[216:217], v20 offset:45056\n\t" \
    "ds_read_b64_tr_b16 v[218:219], v20 offset:45568\n\t" \
    "s_waitcnt vmcnt(7)\n\t" \
    "v_max3_u32 v17, v160, v161, v162\n\t" \
    "v_max_u32 v17, v17, v163\n\t" \
    "v_mul_f32 v71, v71, v79\n\t" \
    "v_mul_f32 v64, v64, v5\n\t" \
    "v_mul_f32 v65, v65, v5\n\t" \
    "v_mfma_f32_32x32x16_bf16 v[112:127], v[196:199], v[144:147], v[112:127]\n\t" \
    "ds_read_b64_tr_b16 v[220:221], v21 offset:45056\n\t" \
    "ds_read_b64_tr_b16 v[222:223], v21 offset:45568\n\t" \
    "v_mul_f32 v66, v66, v5\n\t" \
    "v_mul_f32 v67, v67, v5\n\t" \
    "v_mul_f32 v68, v68, v7\n\t" \
    "s_add_u32 s60, s60, 0x1000\n\t" \
    "buffer_load_dwordx4 v[128:131], v1, s[36:39], s60 offen sc1\n\t" \
    "s_waitcnt lgkmcnt(9)\n\t" \
    "v_mfma_f32_32x32x16_bf16 v[96:111], v[200:203], v[148:151], v[96:111]\n\t" \
    "s_barrier\n\t" \
    "ds_read_b64_tr_b16 v[192:193], v20 offset:16384\n\t" \
    "ds_read_b64_tr_b16 v[194:195], v20 offset:16896\n\t" \
    "s_waitcnt vmcnt(7)\n\t" \
    "v_max3_u32 v17, v164, v165, v17\n\t" \
    "v_max3_u32 v17, v166, v167, v17\n\t" \
    "v_mul_f32 v69, v69, v7\n\t" \
    "v_mul_f32 v70, v70, v7\n\t" \
    "v_mul_f32 v71, v71, v7\n\t" \
    "v_mfma_f32_32x32x16_bf16 v[112:127], v[204:207], v[148:151], v[112:127]\n\t" \
    "ds_read_b64_tr_b16 v[196:197], v21 offset:16384\n\t" \
    "ds_read_b64_tr_b16 v[198:199], v21 offset:16896\n\t" \
    "v_cvt_pk_bf16_f32 v72, v64, v65\n\t" \
    "v_cvt_pk_bf16_f32 v73, v66, v67\n\t" \
    "v_cvt_pk_bf16_f32 v74, v68, v69\n\t" \
    "buffer_load_dwordx4 v[132:135], v1, s[36:39], s60 offen offset:2048 sc1\n\t" \
    "s_waitcnt lgkmcnt(8)\n\t" \
    "v_mfma_f32_32x32x16_bf16 v[96:111], v[208:211], v[152:155], v[96:111]\n\t" \
    "ds_read_b64_tr_b16 v[200:201], v20 offset:20480\n\t" \
    "ds_read_b64_tr_b16 v[202:203], v20 offset:20992\n\t" \
    "s_waitcnt vmcnt(7)\n\t" \
    "v_max3_u32 v17, v168, v169, v17\n\t" \
    "v_max3_u32 v17, v170, v171, v17\n\t" \
    "v_cvt_pk_bf16_f32 v75, v70, v71\n\t" \
    "v_permlane16_swap_b32 v72, v74\n\t" \
    "s_nop 0\n\t" \
    "v_permlane16_swap_b32 v73, v75\n\t" \
    "v_mfma_f32_32x32x16_bf16 v[112:127], v[212:215], v[152:155], v[112:127]\n\t" \
    "ds_read_b64_tr_b16 v[204:205], v21 offset:20480\n\t" \
    "ds_read_b64_tr_b16 v[206:207], v21 offset:20992\n\t" \
    "buffer_store_dwordx4 v[72:75], v1, s[36:39], s61 offen sc1\n\t" \
    "v_fmac_f32 v80, v4, v8\n\t" \
    "v_fmac_f32 v81, v4, v9\n\t" \
    "s_add_u32 s60, s60, 0x1000\n\t" \
    "buffer_load_dwordx4 v[136:139], v1, s[36:39], s60 offen sc1\n\t" \
    "s_waitcnt lgkmcnt(8)\n\t" \
    "v_mfma_f32_32x32x16_bf16 v[96:111], v[216:219], v[156:159], v[96:111]\n\t" \
    "ds_read_b64_tr_b16 v[208:209], v20 offset:24576\n\t" \
    "ds_read_b64_tr_b16 v[210:211], v20 offset:25088\n\t" \
    "s_waitcnt vmcnt(8)\n\t" \
    "v_max3_u32 v17, v172, v173, v17\n\t" \
    "v_max3_u32 v17, v174, v175, v17\n\t" \
    "v_fmac_f32 v82, v4, v10\n\t" \
    "v_fmac_f32 v83, v4, v11\n\t" \
    "v_fmac_f32 v84, v6, v8\n\t" \
    "v_mfma_f32_32x32x16_bf16 v[112:127], v[220:223], v[156:159], v[112:127]\n\t" \
    "ds_read_b64_tr_b16 v[212:213], v21 offset:24576\n\t" \
    "ds_read_b64_tr_b16 v[214:215], v21 offset:25088\n\t" \
    "v_fmac_f32 v85, v6, v9\n\t" \
    "v_fmac_f32 v86, v6, v10\n\t" \
    "v_fmac_f32 v87, v6, v11\n\t" \
    "buffer_load_dwordx4 v[140:143], v1, s[36:39], s60 offen offset:2048 sc1\n\t" \
    "v_cmp_eq_u32 vcc, -1, v17\n\t" \
    "s_and_b64 vcc, exec, vcc\n\t" \
    "s_cbranch_vccnz .Lre2_%=\n\t" \
    ".Lok2_%=:\n\t" \
    "s_waitcnt lgkmcnt(8)\n\t" \
    "v_mfma_f32_32x32x16_bf16 v[96:111], v[192:195], v[160:163], v[96:111]\n\t" \
    "ds_read_b64_tr_b16 v[216:217], v20 offset:28672\n\t" \
    "ds_read_b64_tr_b16 v[218:219], v20 offset:29184\n\t" \
    "s_waitcnt vmcnt(8)\n\t" \
    "v_max3_u32 v17, v176, v177, v178\n\t" \
    "v_max_u32 v17, v17, v179\n\t" \
    "v_fmac_f32 v88, v4, v12\n\t" \
    "v_fmac_f32 v89, v4, v13\n\t" \
    "v_fmac_f32 v90, v4, v14\n\t" \
    "v_mfma_f32_32x32x16_bf16 v[112:127], v[196:199], v[160:163], v[112:127]\n\t" \
    "ds_read_b64_tr_b16 v[220:221], v21 offset:28672\n\t" \
    "ds_read_b64_tr_b16 v[222:223], v21 offset:29184\n\t" \
    "v_fmac_f32 v91, v4, v15\n\t" \
    "v_fmac_f32 v92, v6, v12\n\t" \
    "v_fmac_f32 v93, v6, v13\n\t" \
    "s_add_u32 s60, s60, 0x1000\n\t" \
    "buffer_load_dwordx4 v[144:147], v1, s[36:39], s60 offen sc1\n\t" \
    "s_waitcnt lgkmcnt(8)\n\t" \
    "v_mfma_f32_32x32x16_bf16 v[96:111], v[200:203], v[164:167], v[96:111]\n\t" \
    "s_barrier\n\t" \
    "ds_read_b64_tr_b16 v[192:193], v20 offset:49152\n\t" \
    "ds_read_b64_tr_b16 v[194:195], v20 offset:49664\n\t" \
    "s_waitcnt vmcnt(8)\n\t" \
    "v_max3_u32 v17, v180, v181, v17\n\t" \
    "v_max3_u32 v17, v182, v183, v17\n\t" \
    "v_fmac_f32 v94, v6, v14\n\t" \
    "v_fmac_f32 v95, v6, v15\n\t" \
    "s_add_u32 s61, s45, 0x18000\n\t" \
    "v_mfma_f32_32x32x16_bf16 v[112:127], v[204:207], v[164:167], v[112:127]\n\t" \
    "ds_read_b64_tr_b16 v[196:197], v21 offset:49152\n\t" \
    "ds_read_b64_tr_b16 v[198:199], v21 offset:49664\n\t" \
    "v_mul_f32 v80, v80, v88\n\t" \
    "v_mul_f32 v81, v81, v89\n\t" \
    "v_mul_f32 v82, v82, v90\n\t" \
    "buffer_load_dwordx4 v[148:151], v1, s[36:39], s60 offen offset:2048 sc1\n\t" \
    "s_waitcnt lgkmcnt(8)\n\t" \
    "v_mfma_f32_32x32x16_bf16 v[96:111], v[208:211], v[168:171], v[96:111]\n\t" \
    "ds_read_b64_tr_b16 v[200:201], v20 offset:53248\n\t" \
    "ds_read_b64_tr_b16 v[202:203], v20 offset:53760\n\t" \
    "s_waitcnt vmcnt(8)\n\t" \
    "v_max3_u32 v17, v184, v185, v17\n\t" \
    "v_max3_u32 v17, v186, v187, v17\n\t" \
    "v_mul_f32 v83, v83, v91\n\t" \
    "v_mul_f32 v84, v84, v92\n\t" \
    "v_mul_f32 v85, v85, v93\n\t" \
    "v_mfma_f32_32x32x16_bf16 v[112:127], v[212:215], v[168:171], v[112:127]\n\t" \
    "ds_read_b64_tr_b16 v[204:205], v21 offset:53248\n\t" \
    "ds_read_b64_tr_b16 v[206:207], v21 offset:53760\n\t" \
    "v_mul_f32 v86, v86, v94\n\t" \
    "v_mul_f32 v87, v87, v95\n\t" \
    "v_mul_f32 v80, v80, v5\n\t" \
    "s_add_u32 s60, s60, 0x1000\n\t" \
    "buffer_load_dwordx4 v[152:155], v1, s[36:39], s60 offen sc1\n\t" \
    "s_waitcnt lgkmcnt(8)\n\t" \
    "v_mfma_f32_32x32x16_bf16 v[96:111], v[216:219], v[172:175], v[96:111]\n\t" \
    "ds_read_b64_tr_b16 v[208:209], v20 offset:57344\n\t" \
    "ds_read_b64_tr_b16 v[210:211], v20 offset:57856\n\t" \
    "s_waitcnt vmcnt(8)\n\t" \
    "v_max3_u32 v17, v188, v189, v17\n\t" \
    "v_max3_u32 v17, v190, v191, v17\n\t" \
    "v_mul_f32 v81, v81, v5\n\t" \
    "v_mul_f32 v82, v82, v5\n\t" \
    "v_mul_f32 v83, v83, v5\n\t" \
    "v_mfma_f32_32x32x16_bf16 v[112:127], v[220:223], v[172:175], v[112:127]\n\t" \
    "ds_read_b64_tr_b16 v[212:213], v21 offset:57344\n\t" \
    "ds_read_b64_tr_b16 v[214:215], v21 offset:57856\n\t" \
    "v_mul_f32 v84, v84, v7\n\t" \
    "v_mul_f32 v85, v85, v7\n\t" \
    "v_mul_f32 v86, v86, v7\n\t" \
    "buffer_load_dwordx4 v[156:159], v1, s[36:39], s60 offen offset:2048 sc1\n\t" \
    "v_cmp_eq_u32 vcc, -1, v17\n\t" \
    "s_and_b64 vcc, exec, vcc\n\t" \
    "s_cbranch_vccnz .Lre3_%=\n\t" \
    ".Lok3_%=:\n\t" \
    "s_waitcnt lgkmcnt(8)\n\t" \
    "v_mfma_f32_32x32x16_bf16 v[96:111], v[192:195], v[176:179], v[96:111]\n\t" \
    "ds_read_b64_tr_b16 v[216:217], v20 offset:61440\n\t" \
    "ds_read_b64_tr_b16 v[218:219], v20 offset:61952\n\t" \
    "s_waitcnt vmcnt(8)\n\t" \
    "v_max3_u32 v17, v128, v129, v130\n\t" \
    "v_max_u32 v17, v17, v131\n\t" \
    "v_mul_f32 v87, v87, v7\n\t" \
    "v_cvt_pk_bf16_f32 v88, v80, v81\n\t" \
    "v_cvt_pk_bf16_f32 v89, v82, v83\n\t" \
    "v_mfma_f32_32x32x16_bf16 v[112:127], v[196:199], v[176:179], v[112:127]\n\t" \
    "ds_read_b64_tr_b16 v[220:221], v21 offset:61440\n\t" \
    "ds_read_b64_tr_b16 v[222:223], v21 offset:61952\n\t" \
    "v_cvt_pk_bf16_f32 v90, v84, v85\n\t" \
    "v_cvt_pk_bf16_f32 v91, v86, v87\n\t" \
    "s_nop 0\n\t" \
    "v_permlane16_swap_b32 v88, v90\n\t" \
    "s_add_u32 s60, s60, 0x1000\n\t" \
    "buffer_load_dwordx4 v[160:163], v1, s[36:39], s60 offen sc1\n\t" \
    "s_waitcnt lgkmcnt(8)\n\t" \
    "v_mfma_f32_32x32x16_bf16 v[96:111], v[200:203], v[180:183], v[96:111]\n\t" \
    "s_barrier\n\t" \
    "v_add_u32 v20, 0x10000, v20\n\t" \
    "v_add_u32 v21, 0x10000, v21\n\t" \
    "ds_read_b64_tr_b16 v[192:193], v20 offset:0\n\t" \
    "ds_read_b64_tr_b16 v[194:195], v20 offset:512\n\t" \
    "s_waitcnt vmcnt(8)\n\t" \
    "v_max3_u32 v17, v132, v133, v17\n\t" \
    "v_max3_u32 v17, v134, v135, v17\n\t" \
    "v_permlane16_swap_b32 v89, v91\n\t" \
    "buffer_store_dwordx4 v[88:91], v1, s[36:39], s61 offen sc1\n\t" \
    "v_mfma_f32_32x32x16_bf16 v[112:127], v[204:207], v[180:183], v[112:127]\n\t" \
    "ds_read_b64_tr_b16 v[196:197], v21 offset:0\n\t" \
    "ds_read_b64_tr_b16 v[198:199], v21 offset:512\n\t" \
    "buffer_load_dwordx4 v[164:167], v1, s[36:39], s60 offen offset:2048 sc1\n\t" \
    "s_waitcnt lgkmcnt(8)\n\t" \
    "v_mfma_f32_32x32x16_bf16 v[96:111], v[208:211], v[184:187], v[96:111]\n\t" \
    "ds_read_b64_tr_b16 v[200:201], v20 offset:4096\n\t" \
    "ds_read_b64_tr_b16 v[202:203], v20 offset:4608\n\t" \
    "s_waitcnt vmcnt(8)\n\t" \
    "v_max3_u32 v17, v136, v137, v17\n\t" \
    "v_max3_u32 v17, v138, v139, v17\n\t" \
    "v_mfma_f32_32x32x16_bf16 v[112:127], v[212:215], v[184:187], v[112:127]\n\t" \
    "ds_read_b64_tr_b16 v[204:205], v21 offset:4096\n\t" \
    "ds_read_b64_tr_b16 v[206:207], v21 offset:4608\n\t" \
    "s_add_u32 s60, s60, 0x1000\n\t" \
    "buffer_load_dwordx4 v[168:171], v1, s[36:39], s60 offen sc1\n\t" \
    "s_waitcnt lgkmcnt(8)\n\t" \
    "v_mfma_f32_32x32x16_bf16 v[96:111], v[216:219], v[188:191], v[96:111]\n\t" \
    "ds_read_b64_tr_b16 v[208:209], v20 offset:8192\n\t" \
    "ds_read_b64_tr_b16 v[210:211], v20 offset:8704\n\t" \
    "s_waitcnt vmcnt(8)\n\t" \
    "v_max3_u32 v17, v140, v141, v17\n\t" \
    "v_max3_u32 v17, v142, v143, v17\n\t" \
    "v_mfma_f32_32x32x16_bf16 v[112:127], v[220:223], v[188:191], v[112:127]\n\t" \
    "ds_read_b64_tr_b16 v[212:213], v21 offset:8192\n\t" \
    "ds_read_b64_tr_b16 v[214:215], v21 offset:8704\n\t" \
    "buffer_load_dwordx4 v[172:175], v1, s[36:39], s60 offen offset:2048 sc1\n\t" \
    "v_cmp_eq_u32 vcc, -1, v17\n\t" \
    "s_and_b64 vcc, exec, vcc\n\t" \
    "s_cbranch_vccnz .Lre4_%=\n\t" \
    ".Lok4_%=:\n\t" \
    "s_waitcnt lgkmcnt(8)\n\t" \
    "v_mfma_f32_32x32x16_bf16 v[96:111], v[192:195], v[128:131], v[96:111]\n\t" \
    "ds_read_b64_tr_b16 v[216:217], v20 offset:12288\n\t" \
    "ds_read_b64_tr_b16 v[218:219], v20 offset:12800\n\t" \
    "s_waitcnt vmcnt(8)\n\t" \
    "v_max3_u32 v17, v144, v145, v146\n\t" \
    "v_max_u32 v17, v17, v147\n\t" \
    "v_mfma_f32_32x32x16_bf16 v[112:127], v[196:199], v[128:131], v[112:127]\n\t" \
    "ds_read_b64_tr_b16 v[220:221], v21 offset:12288\n\t" \
    "ds_read_b64_tr_b16 v[222:223], v21 offset:12800\n\t" \
    "s_add_u32 s60, s60, 0x1000\n\t" \
    "buffer_load_dwordx4 v[176:179], v1, s[36:39], s60 offen sc1\n\t" \
    "s_waitcnt lgkmcnt(8)\n\t" \
    "v_mfma_f32_32x32x16_bf16 v[96:111], v[200:203], v[132:135], v[96:111]\n\t" \
    "s_barrier\n\t" \
    "v_add_u32 v20, 0xfffe0000, v20\n\t" \
    "v_add_u32 v21, 0xfffe0000, v21\n\t" \
    "ds_read_b64_tr_b16 v[192:193], v20 offset:0\n\t" \
    "ds_read_b64_tr_b16 v[194:195], v20 offset:512\n\t" \
    "s_waitcnt vmcnt(8)\n\t" \
    "v_max3_u32 v17, v148, v149, v17\n\t" \
    "v_max3_u32 v17, v150, v151, v17\n\t" \
    "v_mfma_f32_32x32x16_bf16 v[112:127], v[204:207], v[132:135], v[112:127]\n\t" \
    "ds_read_b64_tr_b16 v[196:197], v21 offset:0\n\t" \
    "ds_read_b64_tr_b16 v[198:199], v21 offset:512\n\t" \
    "buffer_load_dwordx4 v[180:183], v1, s[36:39], s60 offen offset:2048 sc1\n\t" \
    "s_waitcnt lgkmcnt(8)\n\t" \
    "v_mfma_f32_32x32x16_bf16 v[96:111], v[208:211], v[136:139], v[96:111]\n\t" \
    "ds_read_b64_tr_b16 v[200:201], v20 offset:4096\n\t" \
    "ds_read_b64_tr_b16 v[202:203], v20 offset:4608\n\t" \
    "s_waitcnt vmcnt(8)\n\t" \
    "v_max3_u32 v17, v152, v153, v17\n\t" \
    "v_max3_u32 v17, v154, v155, v17\n\t" \
    "v_mfma_f32_32x32x16_bf16 v[112:127], v[212:215], v[136:139], v[112:127]\n\t" \
    "ds_read_b64_tr_b16 v[204:205], v21 offset:4096\n\t" \
    "ds_read_b64_tr_b16 v[206:207], v21 offset:4608\n\t" \
    "s_add_u32 s60, s60, 0x1000\n\t" \
    "buffer_load_dwordx4 v[184:187], v1, s[36:39], s60 offen sc1\n\t" \
    "s_waitcnt lgkmcnt(8)\n\t" \
    "v_mfma_f32_32x32x16_bf16 v[96:111], v[216:219], v[140:143], v[96:111]\n\t" \
    "ds_read_b64_tr_b16 v[208:209], v20 offset:8192\n\t" \
    "ds_read_b64_tr_b16 v[210:211], v20 offset:8704\n\t" \
    "s_waitcnt vmcnt(8)\n\t" \
    "v_max3_u32 v17, v156, v157, v17\n\t" \
    "v_max3_u32 v17, v158, v159, v17\n\t" \
    "v_mfma_f32_32x32x16_bf16 v[112:127], v[220:223], v[140:143], v[112:127]\n\t" \
    "ds_read_b64_tr_b16 v[212:213], v21 offset:8192\n\t" \
    "ds_read_b64_tr_b16 v[214:215], v21 offset:8704\n\t" \
    "buffer_load_dwordx4 v[188:191], v1, s[36:39], s60 offen offset:2048 sc1\n\t" \
    "v_cmp_eq_u32 vcc, -1, v17\n\t" \
    "s_and_b64 vcc, exec, vcc\n\t" \
    "s_cbranch_vccnz .Lre5_%=\n\t" \
    ".Lok5_%=:\n\t" \
    "s_waitcnt lgkmcnt(8)\n\t" \
    "v_mfma_f32_32x32x16_bf16 v[96:111], v[192:195], v[144:147], v[96:111]\n\t" \
    "ds_read_b64_tr_b16 v[216:217], v20 offset:12288\n\t" \
    "ds_read_b64_tr_b16 v[218:219], v20 offset:12800\n\t" \
    "s_waitcnt vmcnt(8)\n\t" \
    "v_max3_u32 v17, v160, v161, v162\n\t" \
    "v_max_u32 v17, v17, v163\n\t" \
    "v_mfma_f32_32x32x16_bf16 v[112:127], v[196:199], v[144:147], v[112:127]\n\t" \
    "ds_read_b64_tr_b16 v[220:221], v21 offset:12288\n\t" \
    "ds_read_b64_tr_b16 v[222:223], v21 offset:12800\n\t" \
    "s_add_u32 s60, s60, 0x1000\n\t" \
    "buffer_load_dwordx4 v[128:131], v1, s[36:39], s60 offen sc1\n\t" \
    "s_waitcnt lgkmcnt(8)\n\t" \
    "v_mfma_f32_32x32x16_bf16 v[96:111], v[200:203], v[148:151], v[96:111]\n\t" \
    "s_barrier\n\t" \
    "ds_read_b64_tr_b16 v[192:193], v20 offset:16384\n\t" \
    "ds_read_b64_tr_b16 v[194:195], v20 offset:16896\n\t" \
    "s_waitcnt vmcnt(7)\n\t" \
    "v_max3_u32 v17, v164, v165, v17\n\t" \
    "v_max3_u32 v17, v166, v167, v17\n\t" \
    "v_mfma_f32_32x32x16_bf16 v[112:127], v[204:207], v[148:151], v[112:127]\n\t" \
    "ds_read_b64_tr_b16 v[196:197], v21 offset:16384\n\t" \
    "ds_read_b64_tr_b16 v[198:199], v21 offset:16896\n\t" \
    "buffer_load_dwordx4 v[132:135], v1, s[36:39], s60 offen offset:2048 sc1\n\t" \
    "s_waitcnt lgkmcnt(8)\n\t" \
    "v_mfma_f32_32x32x16_bf16 v[96:111], v[208:211], v[152:155], v[96:111]\n\t" \
    "ds_read_b64_tr_b16 v[200:201], v20 offset:20480\n\t" \
    "ds_read_b64_tr_b16 v[202:203], v20 offset:20992\n\t" \
    "s_waitcnt vmcnt(7)\n\t" \
    "v_max3_u32 v17, v168, v169, v17\n\t" \
    "v_max3_u32 v17, v170, v171, v17\n\t" \
    "v_mfma_f32_32x32x16_bf16 v[112:127], v[212:215], v[152:155], v[112:127]\n\t" \
    "ds_read_b64_tr_b16 v[204:205], v21 offset:20480\n\t" \
    "ds_read_b64_tr_b16 v[206:207], v21 offset:20992\n\t" \
    "s_add_u32 s60, s60, 0x1000\n\t" \
    "buffer_load_dwordx4 v[136:139], v1, s[36:39], s60 offen sc1\n\t" \
    "s_waitcnt lgkmcnt(8)\n\t" \
    "v_mfma_f32_32x32x16_bf16 v[96:111], v[216:219], v[156:159], v[96:111]\n\t" \
    "ds_read_b64_tr_b16 v[208:209], v20 offset:24576\n\t" \
    "ds_read_b64_tr_b16 v[210:211], v20 offset:25088\n\t" \
    "s_waitcnt vmcnt(7)\n\t" \
    "v_max3_u32 v17, v172, v173, v17\n\t" \
    "v_max3_u32 v17, v174, v175, v17\n\t" \
    "v_mfma_f32_32x32x16_bf16 v[112:127], v[220:223], v[156:159], v[112:127]\n\t" \
    "ds_read_b64_tr_b16 v[212:213], v21 offset:24576\n\t" \
    "ds_read_b64_tr_b16 v[214:215], v21 offset:25088\n\t" \
    "buffer_load_dwordx4 v[140:143], v1, s[36:39], s60 offen offset:2048 sc1\n\t" \
    "v_cmp_eq_u32 vcc, -1, v17\n\t" \
    "s_and_b64 vcc, exec, vcc\n\t" \
    "s_cbranch_vccnz .Lre6_%=\n\t" \
    ".Lok6_%=:\n\t" \
    "s_waitcnt lgkmcnt(8)\n\t" \
    "v_mfma_f32_32x32x16_bf16 v[96:111], v[192:195], v[160:163], v[96:111]\n\t" \
    "ds_read_b64_tr_b16 v[216:217], v20 offset:28672\n\t" \
    "ds_read_b64_tr_b16 v[218:219], v20 offset:29184\n\t" \
    "s_waitcnt vmcnt(7)\n\t" \
    "v_max3_u32 v17, v176, v177, v178\n\t" \
    "v_max_u32 v17, v17, v179\n\t" \
    "v_mfma_f32_32x32x16_bf16 v[112:127], v[196:199], v[160:163], v[112:127]\n\t" \
    "ds_read_b64_tr_b16 v[220:221], v21 offset:28672\n\t" \
    "ds_read_b64_tr_b16 v[222:223], v21 offset:29184\n\t" \
    "s_add_u32 s60, s60, 0x1000\n\t" \
    "buffer_load_dwordx4 v[144:147], v1, s[36:39], s60 offen sc1\n\t" \
    "s_waitcnt lgkmcnt(8)\n\t" \
    "v_mfma_f32_32x32x16_bf16 v[96:111], v[200:203], v[164:167], v[96:111]\n\t" \
    "s_barrier\n\t" \
    "ds_read_b64_tr_b16 v[192:193], v20 offset:32768\n\t" \
    "ds_read_b64_tr_b16 v[194:195], v20 offset:33280\n\t" \
    "s_waitcnt vmcnt(7)\n\t" \
    "v_max3_u32 v17, v180, v181, v17\n\t" \
    "v_max3_u32 v17, v182, v183, v17\n\t" \
    "v_mfma_f32_32x32x16_bf16 v[112:127], v[204:207], v[164:167], v[112:127]\n\t" \
    "ds_read_b64_tr_b16 v[196:197], v21 offset:32768\n\t" \
    "ds_read_b64_tr_b16 v[198:199], v21 offset:33280\n\t" \
    "buffer_load_dwordx4 v[148:151], v1, s[36:39], s60 offen offset:2048 sc1\n\t" \
    "s_waitcnt lgkmcnt(8)\n\t" \
    "v_mfma_f32_32x32x16_bf16 v[96:111], v[208:211], v[168:171], v[96:111]\n\t" \
    "ds_read_b64_tr_b16 v[200:201], v20 offset:36864\n\t" \
    "ds_read_b64_tr_b16 v[202:203], v20 offset:37376\n\t" \
    "s_waitcnt vmcnt(7)\n\t" \
    "v_max3_u32 v17, v184, v185, v17\n\t" \
    "v_max3_u32 v17, v186, v187, v17\n\t" \
    "v_mfma_f32_32x32x16_bf16 v[112:127], v[212:215], v[168:171], v[112:127]\n\t" \
    "ds_read_b64_tr_b16 v[204:205], v21 offset:36864\n\t" \
    "ds_read_b64_tr_b16 v[206:207], v21 offset:37376\n\t" \
    "s_add_u32 s60, s60, 0x1000\n\t" \
    "buffer_load_dwordx4 v[152:155], v1, s[36:39], s60 offen sc1\n\t" \
    "s_waitcnt lgkmcnt(8)\n\t" \
    "v_mfma_f32_32x32x16_bf16 v[96:111], v[216:219], v[172:175], v[96:111]\n\t" \
    "ds_read_b64_tr_b16 v[208:209], v20 offset:40960\n\t" \
    "ds_read_b64_tr_b16 v[210:211], v20 offset:41472\n\t" \
    "s_waitcnt vmcnt(7)\n\t" \
    "v_max3_u32 v17, v188, v189, v17\n\t" \
    "v_max3_u32 v17, v190, v191, v17\n\t" \
    "v_mfma_f32_32x32x16_bf16 v[112:127], v[220:223], v[172:175], v[112:127]\n\t" \
    "ds_read_b64_tr_b16 v[212:213], v21 offset:40960\n\t" \
    "ds_read_b64_tr_b16 v[214:215], v21 offset:41472\n\t" \
    "buffer_load_dwordx4 v[156:159], v1, s[36:39], s60 offen offset:2048 sc1\n\t" \
    "v_cmp_eq_u32 vcc, -1, v17\n\t" \
    "s_and_b64 vcc, exec, vcc\n\t" \
    "s_cbranch_vccnz .Lre7_%=\n\t" \
    ".Lok7_%=:\n\t" \
    "s_waitcnt lgkmcnt(8)\n\t" \
    "v_mfma_f32_32x32x16_bf16 v[96:111], v[192:195], v[176:179], v[96:111]\n\t" \
    "ds_read_b64_tr_b16 v[216:217], v20 offset:45056\n\t" \
    "ds_read_b64_tr_b16 v[218:219], v20 offset:45568\n\t" \
    "s_waitcnt vmcnt(7)\n\t" \
    "v_max3_u32 v17, v128, v129, v130\n\t" \
    "v_max_u32 v17, v17, v131\n\t" \
    "v_mfma_f32_32x32x16_bf16 v[112:127], v[196:199], v[176:179], v[112:127]\n\t" \
    "ds_read_b64_tr_b16 v[220:221], v21 offset:45056\n\t" \
    "ds_read_b64_tr_b16 v[222:223], v21 offset:45568\n\t" \
    "s_add_u32 s60, s60, 0x1000\n\t" \
    "buffer_load_dwordx4 v[160:163], v1, s[36:39], s60 offen sc1\n\t" \
    "s_waitcnt lgkmcnt(8)\n\t" \
    "v_mfma_f32_32x32x16_bf16 v[96:111], v[200:203], v[180:183], v[96:111]\n\t" \
    "s_barrier\n\t" \
    "ds_read_b64_tr_b16 v[192:193], v20 offset:49152\n\t" \
    "ds_read_b64_tr_b16 v[194:195], v20 offset:49664\n\t" \
    "s_waitcnt vmcnt(7)\n\t" \
    "v_max3_u32 v17, v132, v133, v17\n\t" \
    "v_max3_u32 v17, v134, v135, v17\n\t" \
    "v_mfma_f32_32x32x16_bf16 v[112:127], v[204:207], v[180:183], v[112:127]\n\t" \
    "ds_read_b64_tr_b16 v[196:197], v21 offset:49152\n\t" \
    "ds_read_b64_tr_b16 v[198:199], v21 offset:49664\n\t" \
    "buffer_load_dwordx4 v[164:167], v1, s[36:39], s60 offen offset:2048 sc1\n\t" \
    "s_waitcnt lgkmcnt(8)\n\t" \
    "v_mfma_f32_32x32x16_bf16 v[96:111], v[208:211], v[184:187], v[96:111]\n\t" \
    "ds_read_b64_tr_b16 v[200:201], v20 offset:53248\n\t" \
    "ds_read_b64_tr_b16 v[202:203], v20 offset:53760\n\t" \
    "s_waitcnt vmcnt(7)\n\t" \
    "v_max3_u32 v17, v136, v137, v17\n\t" \
    "v_max3_u32 v17, v138, v139, v17\n\t" \
    "v_mfma_f32_32x32x16_bf16 v[112:127], v[212:215], v[184:187], v[112:127]\n\t" \
    "ds_read_b64_tr_b16 v[204:205], v21 offset:53248\n\t" \
    "ds_read_b64_tr_b16 v[206:207], v21 offset:53760\n\t" \
    "s_add_u32 s60, s60, 0x1000\n\t" \
    "buffer_load_dwordx4 v[168:171], v1, s[36:39], s60 offen sc1\n\t" \
    "s_waitcnt lgkmcnt(8)\n\t" \
    "v_mfma_f32_32x32x16_bf16 v[96:111], v[216:219], v[188:191], v[96:111]\n\t" \
    "ds_read_b64_tr_b16 v[208:209], v20 offset:57344\n\t" \
    "ds_read_b64_tr_b16 v[210:211], v20 offset:57856\n\t" \
    "s_waitcnt vmcnt(7)\n\t" \
    "v_max3_u32 v17, v140, v141, v17\n\t" \
    "v_max3_u32 v17, v142, v143, v17\n\t" \
    "v_mfma_f32_32x32x16_bf16 v[112:127], v[220:223], v[188:191], v[112:127]\n\t" \
    "ds_read_b64_tr_b16 v[212:213], v21 offset:57344\n\t" \
    "ds_read_b64_tr_b16 v[214:215], v21 offset:57856\n\t" \
    "buffer_load_dwordx4 v[172:175], v1, s[36:39], s60 offen offset:2048 sc1\n\t" \
    "v_cmp_eq_u32 vcc, -1, v17\n\t" \
    "s_and_b64 vcc, exec, vcc\n\t" \
    "s_cbranch_vccnz .Lre8_%=\n\t" \
    ".Lok8_%=:\n\t" \
    "s_waitcnt lgkmcnt(8)\n\t" \
    "v_mfma_f32_32x32x16_bf16 v[96:111], v[192:195], v[128:131], v[96:111]\n\t" \
    "ds_read_b64_tr_b16 v[216:217], v20 offset:61440\n\t" \
    "ds_read_b64_tr_b16 v[218:219], v20 offset:61952\n\t" \
    "s_waitcnt vmcnt(7)\n\t" \
    "v_max3_u32 v17, v144, v145, v146\n\t" \
    "v_max_u32 v17, v17, v147\n\t" \
    "v_mfma_f32_32x32x16_bf16 v[112:127], v[196:199], v[128:131], v[112:127]\n\t" \
    "ds_read_b64_tr_b16 v[220:221], v21 offset:61440\n\t" \
    "ds_read_b64_tr_b16 v[222:223], v21 offset:61952\n\t" \
    "s_add_u32 s60, s60, 0x1000\n\t" \
    "buffer_load_dwordx4 v[176:179], v1, s[36:39], s60 offen sc1\n\t" \
    "s_waitcnt lgkmcnt(8)\n\t" \
    "v_mfma_f32_32x32x16_bf16 v[96:111], v[200:203], v[132:135], v[96:111]\n\t" \
    "s_barrier\n\t" \
    "v_add_u32 v20, 0x10000, v20\n\t" \
    "v_add_u32 v21, 0x10000, v21\n\t" \
    "ds_read_b64_tr_b16 v[192:193], v20 offset:0\n\t" \
    "ds_read_b64_tr_b16 v[194:195], v20 offset:512\n\t" \
    "s_waitcnt vmcnt(7)\n\t" \
    "v_max3_u32 v17, v148, v149, v17\n\t" \
    "v_max3_u32 v17, v150, v151, v17\n\t" \
    "v_mfma_f32_32x32x16_bf16 v[112:127], v[204:207], v[132:135], v[112:127]\n\t" \
    "ds_read_b64_tr_b16 v[196:197], v21 offset:0\n\t" \
    "ds_read_b64_tr_b16 v[198:199], v21 offset:512\n\t" \
    "buffer_load_dwordx4 v[180:183], v1, s[36:39], s60 offen offset:2048 sc1\n\t" \
    "s_waitcnt lgkmcnt(8)\n\t" \
    "v_mfma_f32_32x32x16_bf16 v[96:111], v[208:211], v[136:139], v[96:111]\n\t" \
    "ds_read_b64_tr_b16 v[200:201], v20 offset:4096\n\t" \
    "ds_read_b64_tr_b16 v[202:203], v20 offset:4608\n\t" \
    "s_waitcnt vmcnt(7)\n\t" \
    "v_max3_u32 v17, v152, v153, v17\n\t" \
    "v_max3_u32 v17, v154, v155, v17\n\t" \
    "v_mfma_f32_32x32x16_bf16 v[112:127], v[212:215], v[136:139], v[112:127]\n\t" \
    "ds_read_b64_tr_b16 v[204:205], v21 offset:4096\n\t" \
    "ds_read_b64_tr_b16 v[206:207], v21 offset:4608\n\t" \
    "s_add_u32 s60, s60, 0x1000\n\t" \
    "buffer_load_dwordx4 v[184:187], v1, s[36:39], s60 offen sc1\n\t" \
    "s_waitcnt lgkmcnt(8)\n\t" \
    "v_mfma_f32_32x32x16_bf16 v[96:111], v[216:219], v[140:143], v[96:111]\n\t" \
    "ds_read_b64_tr_b16 v[208:209], v20 offset:8192\n\t" \
    "ds_read_b64_tr_b16 v[210:211], v20 offset:8704\n\t" \
    "s_waitcnt vmcnt(7)\n\t" \
    "v_max3_u32 v17, v156, v157, v17\n\t" \
    "v_max3_u32 v17, v158, v159, v17\n\t" \
    "v_mfma_f32_32x32x16_bf16 v[112:127], v[220:223], v[140:143], v[112:127]\n\t" \
    "ds_read_b64_tr_b16 v[212:213], v21 offset:8192\n\t" \
    "ds_read_b64_tr_b16 v[214:215], v21 offset:8704\n\t" \
    "buffer_load_dwordx4 v[188:191], v1, s[36:39], s60 offen offset:2048 sc1\n\t" \
    "v_cmp_eq_u32 vcc, -1, v17\n\t" \
    "s_and_b64 vcc, exec, vcc\n\t" \
    "s_cbranch_vccnz .Lre9_%=\n\t" \
    ".Lok9_%=:\n\t" \
    "s_waitcnt lgkmcnt(8)\n\t" \
    "v_mfma_f32_32x32x16_bf16 v[96:111], v[192:195], v[144:147], v[96:111]\n\t" \
    "ds_read_b64_tr_b16 v[216:217], v20 offset:12288\n\t" \
    "ds_read_b64_tr_b16 v[218:219], v20 offset:12800\n\t" \
    "s_waitcnt vmcnt(7)\n\t" \
    "v_max3_u32 v17, v160, v161, v162\n\t" \
    "v_max_u32 v17, v17, v163\n\t" \
    "v_mfma_f32_32x32x16_bf16 v[112:127], v[196:199], v[144:147], v[112:127]\n\t" \
    "ds_read_b64_tr_b16 v[220:221], v21 offset:12288\n\t" \
    "ds_read_b64_tr_b16 v[222:223], v21 offset:12800\n\t" \
    "s_add_u32 s60, s60, 0x1000\n\t" \
    "buffer_load_dwordx4 v[128:131], v1, s[36:39], s60 offen sc1\n\t" \
    "s_waitcnt lgkmcnt(8)\n\t" \
    "v_mfma_f32_32x32x16_bf16 v[96:111], v[200:203], v[148:151], v[96:111]\n\t" \
    "s_barrier\n\t" \
    "ds_read_b64_tr_b16 v[192:193], v20 offset:32768\n\t" \
    "ds_read_b64_tr_b16 v[194:195], v20 offset:33280\n\t" \
    "s_waitcnt vmcnt(7)\n\t" \
    "v_max3_u32 v17, v164, v165, v17\n\t" \
    "v_max3_u32 v17, v166, v167, v17\n\t" \
    "v_mfma_f32_32x32x16_bf16 v[112:127], v[204:207], v[148:151], v[112:127]\n\t" \
    "ds_read_b64_tr_b16 v[196:197], v21 offset:32768\n\t" \
    "ds_read_b64_tr_b16 v[198:199], v21 offset:33280\n\t" \
    "buffer_load_dwordx4 v[132:135], v1, s[36:39], s60 offen offset:2048 sc1\n\t" \
    "s_waitcnt lgkmcnt(8)\n\t" \
    "v_mfma_f32_32x32x16_bf16 v[96:111], v[208:211], v[152:155], v[96:111]\n\t" \
    "ds_read_b64_tr_b16 v[200:201], v20 offset:36864\n\t" \
    "ds_read_b64_tr_b16 v[202:203], v20 offset:37376\n\t" \
    "s_waitcnt vmcnt(7)\n\t" \
    "v_max3_u32 v17, v168, v169, v17\n\t" \
    "v_max3_u32 v17, v170, v171, v17\n\t" \
    "v_mfma_f32_32x32x16_bf16 v[112:127], v[212:215], v[152:155], v[112:127]\n\t" \
    "ds_read_b64_tr_b16 v[204:205], v21 offset:36864\n\t" \
    "ds_read_b64_tr_b16 v[206:207], v21 offset:37376\n\t" \
    "s_add_u32 s60, s60, 0x1000\n\t" \
    "buffer_load_dwordx4 v[136:139], v1, s[36:39], s60 offen sc1\n\t" \
    "s_waitcnt lgkmcnt(8)\n\t" \
    "v_mfma_f32_32x32x16_bf16 v[96:111], v[216:219], v[156:159], v[96:111]\n\t" \
    "ds_read_b64_tr_b16 v[208:209], v20 offset:40960\n\t" \
    "ds_read_b64_tr_b16 v[210:211], v20 offset:41472\n\t" \
    "s_waitcnt vmcnt(7)\n\t" \
    "v_max3_u32 v17, v172, v173, v17\n\t" \
    "v_max3_u32 v17, v174, v175, v17\n\t" \
    "v_mfma_f32_32x32x16_bf16 v[112:127], v[220:223], v[156:159], v[112:127]\n\t" \
    "ds_read_b64_tr_b16 v[212:213], v21 offset:40960\n\t" \
    "ds_read_b64_tr_b16 v[214:215], v21 offset:41472\n\t" \
    "buffer_load_dwordx4 v[140:143], v1, s[36:39], s60 offen offset:2048 sc1\n\t" \
    "v_cmp_eq_u32 vcc, -1, v17\n\t" \
    "s_and_b64 vcc, exec, vcc\n\t" \
    "s_cbranch_vccnz .Lre10_%=\n\t" \
    ".Lok10_%=:\n\t" \
    "s_waitcnt lgkmcnt(8)\n\t" \
    "v_mfma_f32_32x32x16_bf16 v[96:111], v[192:195], v[160:163], v[96:111]\n\t" \
    "ds_read_b64_tr_b16 v[216:217], v20 offset:45056\n\t" \
    "ds_read_b64_tr_b16 v[218:219], v20 offset:45568\n\t" \
    "s_waitcnt vmcnt(7)\n\t" \
    "v_max3_u32 v17, v176, v177, v178\n\t" \
    "v_max_u32 v17, v17, v179\n\t" \
    "v_mfma_f32_32x32x16_bf16 v[112:127], v[196:199], v[160:163], v[112:127]\n\t" \
    "ds_read_b64_tr_b16 v[220:221], v21 offset:45056\n\t" \
    "ds_read_b64_tr_b16 v[222:223], v21 offset:45568\n\t" \
    "s_add_u32 s60, s60, 0x1000\n\t" \
    "buffer_load_dwordx4 v[144:147], v1, s[36:39], s60 offen sc1\n\t" \
    "s_waitcnt lgkmcnt(8)\n\t" \
    "v_mfma_f32_32x32x16_bf16 v[96:111], v[200:203], v[164:167], v[96:111]\n\t" \
    "s_barrier\n\t" \
    "ds_read_b64_tr_b16 v[192:193], v20 offset:16384\n\t" \
    "ds_read_b64_tr_b16 v[194:195], v20 offset:16896\n\t" \
    "s_waitcnt vmcnt(7)\n\t" \
    "v_max3_u32 v17, v180, v181, v17\n\t" \
    "v_max3_u32 v17, v182, v183, v17\n\t" \
    "v_mfma_f32_32x32x16_bf16 v[112:127], v[204:207], v[164:167], v[112:127]\n\t" \
    "ds_read_b64_tr_b16 v[196:197], v21 offset:16384\n\t" \
    "ds_read_b64_tr_b16 v[198:199], v21 offset:16896\n\t" \
    "buffer_load_dwordx4 v[148:151], v1, s[36:39], s60 offen offset:2048 sc1\n\t" \
    "s_waitcnt lgkmcnt(8)\n\t" \
    "v_mfma_f32_32x32x16_bf16 v[96:111], v[208:211], v[168:171], v[96:111]\n\t" \
    "ds_read_b64_tr_b16 v[200:201], v20 offset:20480\n\t" \
    "ds_read_b64_tr_b16 v[202:203], v20 offset:20992\n\t" \
    "s_waitcnt vmcnt(7)\n\t" \
    "v_max3_u32 v17, v184, v185, v17\n\t" \
    "v_max3_u32 v17, v186, v187, v17\n\t" \
    "v_mfma_f32_32x32x16_bf16 v[112:127], v[212:215], v[168:171], v[112:127]\n\t" \
    "ds_read_b64_tr_b16 v[204:205], v21 offset:20480\n\t" \
    "ds_read_b64_tr_b16 v[206:207], v21 offset:20992\n\t" \
    "s_add_u32 s60, s60, 0x1000\n\t" \
    "buffer_load_dwordx4 v[152:155], v1, s[36:39], s60 offen sc1\n\t" \
    "s_waitcnt lgkmcnt(8)\n\t" \
    "v_mfma_f32_32x32x16_bf16 v[96:111], v[216:219], v[172:175], v[96:111]\n\t" \
    "ds_read_b64_tr_b16 v[208:209], v20 offset:24576\n\t" \
    "ds_read_b64_tr_b16 v[210:211], v20 offset:25088\n\t" \
    "s_waitcnt vmcnt(7)\n\t" \
    "v_max3_u32 v17, v188, v189, v17\n\t" \
    "v_max3_u32 v17, v190, v191, v17\n\t" \
    "v_mfma_f32_32x32x16_bf16 v[112:127], v[220:223], v[172:175], v[112:127]\n\t" \
    "ds_read_b64_tr_b16 v[212:213], v21 offset:24576\n\t" \
    "ds_read_b64_tr_b16 v[214:215], v21 offset:25088\n\t" \
    "buffer_load_dwordx4 v[156:159], v1, s[36:39], s60 offen offset:2048 sc1\n\t" \
    "v_cmp_eq_u32 vcc, -1, v17\n\t" \
    "s_and_b64 vcc, exec, vcc\n\t" \
    "s_cbranch_vccnz .Lre11_%=\n\t" \
    ".Lok11_%=:\n\t" \
    "s_waitcnt lgkmcnt(8)\n\t" \
    "v_mfma_f32_32x32x16_bf16 v[96:111], v[192:195], v[176:179], v[96:111]\n\t" \
    "ds_read_b64_tr_b16 v[216:217], v20 offset:28672\n\t" \
    "ds_read_b64_tr_b16 v[218:219], v20 offset:29184\n\t" \
    "s_waitcnt vmcnt(7)\n\t" \
    "v_max3_u32 v17, v128, v129, v130\n\t" \
    "v_max_u32 v17, v17, v131\n\t" \
    "v_mfma_f32_32x32x16_bf16 v[112:127], v[196:199], v[176:179], v[112:127]\n\t" \
    "ds_read_b64_tr_b16 v[220:221], v21 offset:28672\n\t" \
    "ds_read_b64_tr_b16 v[222:223], v21 offset:29184\n\t" \
    "s_waitcnt lgkmcnt(8)\n\t" \
    "v_mfma_f32_32x32x16_bf16 v[96:111], v[200:203], v[180:183], v[96:111]\n\t" \
    "s_barrier\n\t" \
    "ds_read_b64_tr_b16 v[192:193], v21 offset:49152\n\t" \
    "ds_read_b64_tr_b16 v[194:195], v21 offset:49664\n\t" \
    "s_waitcnt vmcnt(6)\n\t" \
    "v_max3_u32 v17, v132, v133, v17\n\t" \
    "v_max3_u32 v17, v134, v135, v17\n\t" \
    "v_mfma_f32_32x32x16_bf16 v[112:127], v[204:207], v[180:183], v[112:127]\n\t" \
    "ds_read_b64_tr_b16 v[196:197], v21 offset:53248\n\t" \
    "ds_read_b64_tr_b16 v[198:199], v21 offset:53760\n\t" \
    "s_waitcnt lgkmcnt(8)\n\t" \
    "v_mfma_f32_32x32x16_bf16 v[96:111], v[208:211], v[184:187], v[96:111]\n\t" \
    "ds_read_b64_tr_b16 v[200:201], v21 offset:57344\n\t" \
    "ds_read_b64_tr_b16 v[202:203], v21 offset:57856\n\t" \
    "s_waitcnt vmcnt(5)\n\t" \
    "v_max3_u32 v17, v136, v137, v17\n\t" \
    "v_max3_u32 v17, v138, v139, v17\n\t" \
    "v_mfma_f32_32x32x16_bf16 v[112:127], v[212:215], v[184:187], v[112:127]\n\t" \
    "ds_read_b64_tr_b16 v[204:205], v21 offset:61440\n\t" \
    "ds_read_b64_tr_b16 v[206:207], v21 offset:61952\n\t" \
    "s_waitcnt lgkmcnt(8)\n\t" \
    "v_mfma_f32_32x32x16_bf16 v[96:111], v[216:219], v[188:191], v[96:111]\n\t" \
    "s_barrier\n\t" \
    "v_add_u32 v20, 0x10000, v20\n\t" \
    "v_add_u32 v21, 0x10000, v21\n\t" \
    "ds_read_b64_tr_b16 v[208:209], v21 offset:0\n\t" \
    "ds_read_b64_tr_b16 v[210:211], v21 offset:512\n\t" \
    "s_waitcnt vmcnt(4)\n\t" \
    "v_max3_u32 v17, v140, v141, v17\n\t" \
    "v_max3_u32 v17, v142, v143, v17\n\t" \
    "v_mfma_f32_32x32x16_bf16 v[112:127], v[220:223], v[188:191], v[112:127]\n\t" \
    "ds_read_b64_tr_b16 v[212:213], v21 offset:4096\n\t" \
    "ds_read_b64_tr_b16 v[214:215], v21 offset:4608\n\t" \
    "v_cmp_eq_u32 vcc, -1, v17\n\t" \
    "s_and_b64 vcc, exec, vcc\n\t" \
    "s_cbranch_vccnz .Lre12_%=\n\t" \
    ".Lok12_%=:\n\t" \
    "s_waitcnt lgkmcnt(8)\n\t" \
    "v_mfma_f32_32x32x16_bf16 v[112:127], v[192:195], v[128:131], v[112:127]\n\t" \
    "ds_read_b64_tr_b16 v[216:217], v21 offset:8192\n\t" \
    "ds_read_b64_tr_b16 v[218:219], v21 offset:8704\n\t" \
    "s_waitcnt vmcnt(3)\n\t" \
    "v_max3_u32 v17, v144, v145, v146\n\t" \
    "v_max_u32 v17, v17, v147\n\t" \
    "v_mfma_f32_32x32x16_bf16 v[112:127], v[196:199], v[132:135], v[112:127]\n\t" \
    "ds_read_b64_tr_b16 v[220:221], v21 offset:12288\n\t" \
    "ds_read_b64_tr_b16 v[222:223], v21 offset:12800\n\t" \
    "s_waitcnt vmcnt(2)\n\t" \
    "v_max3_u32 v17, v148, v149, v17\n\t" \
    "v_max3_u32 v17, v150, v151, v17\n\t" \
    "s_waitcnt lgkmcnt(8)\n\t" \
    "v_mfma_f32_32x32x16_bf16 v[112:127], v[200:203], v[136:139], v[112:127]\n\t" \
    "v_add_u32 v20, 0xffff0000, v20\n\t" \
    "v_add_u32 v21, 0xffff0000, v21\n\t" \
    "ds_read_b64_tr_b16 v[192:193], v20 offset:49152\n\t" \
    "ds_read_b64_tr_b16 v[194:195], v20 offset:49664\n\t" \
    "s_waitcnt vmcnt(1)\n\t" \
    "v_max3_u32 v17, v152, v153, v17\n\t" \
    "v_max3_u32 v17, v154, v155, v17\n\t" \
    "v_mfma_f32_32x32x16_bf16 v[112:127], v[204:207], v[140:143], v[112:127]\n\t" \
    "ds_read_b64_tr_b16 v[196:197], v20 offset:53248\n\t" \
    "ds_read_b64_tr_b16 v[198:199], v20 offset:53760\n\t" \
    "s_waitcnt vmcnt(0)\n\t" \
    "v_max3_u32 v17, v156, v157, v17\n\t" \
    "v_max3_u32 v17, v158, v159, v17\n\t" \
    "v_cmp_eq_u32 vcc, -1, v17\n\t" \
    "s_and_b64 vcc, exec, vcc\n\t" \
    "s_cbranch_vccnz .Lre13_%=\n\t" \
    ".Lok13_%=:\n\t" \
    "s_waitcnt lgkmcnt(8)\n\t" \
    "v_mfma_f32_32x32x16_bf16 v[112:127], v[208:211], v[144:147], v[112:127]\n\t" \
    "ds_read_b64_tr_b16 v[200:201], v20 offset:57344\n\t" \
    "ds_read_b64_tr_b16 v[202:203], v20 offset:57856\n\t" \
    "v_mfma_f32_32x32x16_bf16 v[112:127], v[212:215], v[148:151], v[112:127]\n\t" \
    "ds_read_b64_tr_b16 v[204:205], v20 offset:61440\n\t" \
    "ds_read_b64_tr_b16 v[206:207], v20 offset:61952\n\t" \
    "s_waitcnt lgkmcnt(8)\n\t" \
    "v_mfma_f32_32x32x16_bf16 v[112:127], v[216:219], v[152:155], v[112:127]\n\t" \
    "v_add_u32 v20, 0x10000, v20\n\t" \
    "v_add_u32 v21, 0x10000, v21\n\t" \
    "ds_read_b64_tr_b16 v[208:209], v20 offset:0\n\t" \
    "ds_read_b64_tr_b16 v[210:211], v20 offset:512\n\t" \
    "v_mfma_f32_32x32x16_bf16 v[112:127], v[220:223], v[156:159], v[112:127]\n\t" \
    "ds_read_b64_tr_b16 v[212:213], v20 offset:4096\n\t" \
    "ds_read_b64_tr_b16 v[214:215], v20 offset:4608\n\t" \
    "s_waitcnt lgkmcnt(8)\n\t" \
    "v_mfma_f32_32x32x16_bf16 v[96:111], v[192:195], v[128:131], v[96:111]\n\t" \
    "ds_read_b64_tr_b16 v[216:217], v20 offset:8192\n\t" \
    "ds_read_b64_tr_b16 v[218:219], v20 offset:8704\n\t" \
    "v_mfma_f32_32x32x16_bf16 v[96:111], v[196:199], v[132:135], v[96:111]\n\t" \
    "ds_read_b64_tr_b16 v[220:221], v20 offset:12288\n\t" \
    "ds_read_b64_tr_b16 v[222:223], v20 offset:12800\n\t" \
    "s_nop 2\n\t" \
    "ds_write_b128 v19, v[112:115] offset:0\n\t" \
    "s_waitcnt lgkmcnt(9)\n\t" \
    "v_mfma_f32_32x32x16_bf16 v[96:111], v[200:203], v[136:139], v[96:111]\n\t" \
    "ds_write_b128 v19, v[116:119] offset:1024\n\t" \
    "v_mfma_f32_32x32x16_bf16 v[96:111], v[204:207], v[140:143], v[96:111]\n\t" \
    "ds_write_b128 v19, v[120:123] offset:2048\n\t" \
    "s_waitcnt lgkmcnt(7)\n\t" \
    "v_mfma_f32_32x32x16_bf16 v[96:111], v[208:211], v[144:147], v[96:111]\n\t" \
    "ds_write_b128 v19, v[124:127] offset:3072\n\t" \
    "v_mfma_f32_32x32x16_bf16 v[96:111], v[212:215], v[148:151], v[96:111]\n\t" \
    "s_waitcnt lgkmcnt(4)\n\t" \
    "v_mfma_f32_32x32x16_bf16 v[96:111], v[216:219], v[152:155], v[96:111]\n\t" \
    "s_waitcnt lgkmcnt(0)\n\t" \
    "s_barrier\n\t" \
    "ds_read_b128 v[112:115], v22 offset:0\n\t" \
    "ds_read_b128 v[116:119], v22 offset:1024\n\t" \
    "ds_read_b128 v[120:123], v22 offset:2048\n\t" \
    "ds_read_b128 v[124:127], v22 offset:3072\n\t" \
    "v_mfma_f32_32x32x16_bf16 v[96:111], v[220:223], v[156:159], v[96:111]\n\t" \
    "s_waitcnt lgkmcnt(3)\n\t" \
    "s_nop 10\n\t" \
    "v_add_f32 v96, v96, v112\n\t" \
    "v_add_f32 v97, v97, v113\n\t" \
    "v_add_f32 v98, v98, v114\n\t" \
    "v_add_f32 v99, v99, v115\n\t" \
    "v_cvt_pk_bf16_f32 v24, v96, v97\n\t" \
    "v_cvt_pk_bf16_f32 v25, v98, v99\n\t" \
    "s_waitcnt lgkmcnt(2)\n\t" \
    "v_add_f32 v100, v100, v116\n\t" \
    "v_add_f32 v101, v101, v117\n\t" \
    "v_add_f32 v102, v102, v118\n\t" \
    "v_add_f32 v103, v103, v119\n\t" \
    "v_cvt_pk_bf16_f32 v26, v100, v101\n\t" \
    "v_cvt_pk_bf16_f32 v27, v102, v103\n\t" \
    "s_nop 0\n\t" \
    "v_permlane32_swap_b32 v24, v26\n\t" \
    "v_permlane32_swap_b32 v25, v27\n\t" \
    "buffer_store_dwordx4 v[24:27], v3, s[40:43], s48 offen\n\t" \
    "s_waitcnt lgkmcnt(1)\n\t" \
    "v_add_f32 v104, v104, v120\n\t" \
    "v_add_f32 v105, v105, v121\n\t" \
    "v_add_f32 v106, v106, v122\n\t" \
    "v_add_f32 v107, v107, v123\n\t" \
    "v_cvt_pk_bf16_f32 v28, v104, v105\n\t" \
    "v_cvt_pk_bf16_f32 v29, v106, v107\n\t" \
    "s_waitcnt lgkmcnt(0)\n\t" \
    "v_add_f32 v108, v108, v124\n\t" \
    "v_add_f32 v109, v109, v125\n\t" \
    "v_add_f32 v110, v110, v126\n\t" \
    "v_add_f32 v111, v111, v127\n\t" \
    "v_cvt_pk_bf16_f32 v30, v108, v109\n\t" \
    "v_cvt_pk_bf16_f32 v31, v110, v111\n\t" \
    "s_nop 0\n\t" \
    "v_permlane32_swap_b32 v28, v30\n\t" \
    "v_permlane32_swap_b32 v29, v31\n\t" \
    "buffer_store_dwordx4 v[28:31], v3, s[40:43], s48 offen offset:32\n\t" \
    "s_endpgm\n\t" \
    ".Lre0_%=:\n\t" \
    "s_mov_b32 s63, 0x20000\n\t" \
    "s_add_u32 s50, s46, 0x0\n\t" \
    "s_add_u32 s51, s50, 0x1000\n\t" \
    ".Lrf0_%=:\n\t" \
    "buffer_load_dwordx4 v[128:131], v1, s[36:39], s50 offen sc1\n\t" \
    "buffer_load_dwordx4 v[132:135], v1, s[36:39], s50 offen offset:2048 sc1\n\t" \
    "buffer_load_dwordx4 v[136:139], v1, s[36:39], s51 offen sc1\n\t" \
    "buffer_load_dwordx4 v[140:143], v1, s[36:39], s51 offen offset:2048 sc1\n\t" \
    "s_waitcnt vmcnt(0)\n\t" \
    "v_max3_u32 v17, v128, v129, v130\n\t" \
    "v_max_u32 v17, v17, v131\n\t" \
    "v_max3_u32 v17, v132, v133, v17\n\t" \
    "v_max3_u32 v17, v134, v135, v17\n\t" \
    "v_max3_u32 v17, v136, v137, v17\n\t" \
    "v_max3_u32 v17, v138, v139, v17\n\t" \
    "v_max3_u32 v17, v140, v141, v17\n\t" \
    "v_max3_u32 v17, v142, v143, v17\n\t" \
    "v_cmp_eq_u32 vcc, -1, v17\n\t" \
    "s_sub_u32 s63, s63, 1\n\t" \
    "s_cbranch_scc1 .Lok0_%=\n\t" \
    "s_and_b64 vcc, exec, vcc\n\t" \
    "s_cbranch_vccnz .Lrf0_%=\n\t" \
    "s_branch .Lok0_%=\n\t" \
    ".Lre1_%=:\n\t" \
    "s_mov_b32 s63, 0x20000\n\t" \
    "s_add_u32 s50, s46, 0x2000\n\t" \
    "s_add_u32 s51, s50, 0x1000\n\t" \
    ".Lrf1_%=:\n\t" \
    "buffer_load_dwordx4 v[144:147], v1, s[36:39], s50 offen sc1\n\t" \
    "buffer_load_dwordx4 v[148:151], v1, s[36:39], s50 offen offset:2048 sc1\n\t" \
    "buffer_load_dwordx4 v[152:155], v1, s[36:39], s51 offen sc1\n\t" \
    "buffer_load_dwordx4 v[156:159], v1, s[36:39], s51 offen offset:2048 sc1\n\t" \
    "s_waitcnt vmcnt(0)\n\t" \
    "v_max3_u32 v17, v144, v145, v146\n\t" \
    "v_max_u32 v17, v17, v147\n\t" \
    "v_max3_u32 v17, v148, v149, v17\n\t" \
    "v_max3_u32 v17, v150, v151, v17\n\t" \
    "v_max3_u32 v17, v152, v153, v17\n\t" \
    "v_max3_u32 v17, v154, v155, v17\n\t" \
    "v_max3_u32 v17, v156, v157, v17\n\t" \
    "v_max3_u32 v17, v158, v159, v17\n\t" \
    "v_cmp_eq_u32 vcc, -1, v17\n\t" \
    "s_sub_u32 s63, s63, 1\n\t" \
    "s_cbranch_scc1 .Lok1_%=\n\t" \
    "s_and_b64 vcc, exec, vcc\n\t" \
    "s_cbranch_vccnz .Lrf1_%=\n\t" \
    "s_branch .Lok1_%=\n\t" \
    ".Lre2_%=:\n\t" \
    "s_mov_b32 s63, 0x20000\n\t" \
    "s_add_u32 s50, s46, 0x4000\n\t" \
    "s_add_u32 s51, s50, 0x1000\n\t" \
    ".Lrf2_%=:\n\t" \
    "buffer_load_dwordx4 v[160:163], v1, s[36:39], s50 offen sc1\n\t" \
    "buffer_load_dwordx4 v[164:167], v1, s[36:39], s50 offen offset:2048 sc1\n\t" \
    "buffer_load_dwordx4 v[168:171], v1, s[36:39], s51 offen sc1\n\t" \
    "buffer_load_dwordx4 v[172:175], v1, s[36:39], s51 offen offset:2048 sc1\n\t" \
    "s_waitcnt vmcnt(0)\n\t" \
    "v_max3_u32 v17, v160, v161, v162\n\t" \
    "v_max_u32 v17, v17, v163\n\t" \
    "v_max3_u32 v17, v164, v165, v17\n\t" \
    "v_max3_u32 v17, v166, v167, v17\n\t" \
    "v_max3_u32 v17, v168, v169, v17\n\t" \
    "v_max3_u32 v17, v170, v171, v17\n\t" \
    "v_max3_u32 v17, v172, v173, v17\n\t" \
    "v_max3_u32 v17, v174, v175, v17\n\t" \
    "v_cmp_eq_u32 vcc, -1, v17\n\t" \
    "s_sub_u32 s63, s63, 1\n\t" \
    "s_cbranch_scc1 .Lok2_%=\n\t" \
    "s_and_b64 vcc, exec, vcc\n\t" \
    "s_cbranch_vccnz .Lrf2_%=\n\t" \
    "s_branch .Lok2_%=\n\t" \
    ".Lre3_%=:\n\t" \
    "s_mov_b32 s63, 0x20000\n\t" \
    "s_add_u32 s50, s46, 0x6000\n\t" \
    "s_add_u32 s51, s50, 0x1000\n\t" \
    ".Lrf3_%=:\n\t" \
    "buffer_load_dwordx4 v[176:179], v1, s[36:39], s50 offen sc1\n\t" \
    "buffer_load_dwordx4 v[180:183], v1, s[36:39], s50 offen offset:2048 sc1\n\t" \
    "buffer_load_dwordx4 v[184:187], v1, s[36:39], s51 offen sc1\n\t" \
    "buffer_load_dwordx4 v[188:191], v1, s[36:39], s51 offen offset:2048 sc1\n\t" \
    "s_waitcnt vmcnt(0)\n\t" \
    "v_max3_u32 v17, v176, v177, v178\n\t" \
    "v_max_u32 v17, v17, v179\n\t" \
    "v_max3_u32 v17, v180, v181, v17\n\t" \
    "v_max3_u32 v17, v182, v183, v17\n\t" \
    "v_max3_u32 v17, v184, v185, v17\n\t" \
    "v_max3_u32 v17, v186, v187, v17\n\t" \
    "v_max3_u32 v17, v188, v189, v17\n\t" \
    "v_max3_u32 v17, v190, v191, v17\n\t" \
    "v_cmp_eq_u32 vcc, -1, v17\n\t" \
    "s_sub_u32 s63, s63, 1\n\t" \
    "s_cbranch_scc1 .Lok3_%=\n\t" \
    "s_and_b64 vcc, exec, vcc\n\t" \
    "s_cbranch_vccnz .Lrf3_%=\n\t" \
    "s_branch .Lok3_%=\n\t" \
    ".Lre4_%=:\n\t" \
    "s_mov_b32 s63, 0x20000\n\t" \
    "s_add_u32 s50, s46, 0x8000\n\t" \
    "s_add_u32 s51, s50, 0x1000\n\t" \
    ".Lrf4_%=:\n\t" \
    "buffer_load_dwordx4 v[128:131], v1, s[36:39], s50 offen sc1\n\t" \
    "buffer_load_dwordx4 v[132:135], v1, s[36:39], s50 offen offset:2048 sc1\n\t" \
    "buffer_load_dwordx4 v[136:139], v1, s[36:39], s51 offen sc1\n\t" \
    "buffer_load_dwordx4 v[140:143], v1, s[36:39], s51 offen offset:2048 sc1\n\t" \
    "s_waitcnt vmcnt(0)\n\t" \
    "v_max3_u32 v17, v128, v129, v130\n\t" \
    "v_max_u32 v17, v17, v131\n\t" \
    "v_max3_u32 v17, v132, v133, v17\n\t" \
    "v_max3_u32 v17, v134, v135, v17\n\t" \
    "v_max3_u32 v17, v136, v137, v17\n\t" \
    "v_max3_u32 v17, v138, v139, v17\n\t" \
    "v_max3_u32 v17, v140, v141, v17\n\t" \
    "v_max3_u32 v17, v142, v143, v17\n\t" \
    "v_cmp_eq_u32 vcc, -1, v17\n\t" \
    "s_sub_u32 s63, s63, 1\n\t" \
    "s_cbranch_scc1 .Lok4_%=\n\t" \
    "s_and_b64 vcc, exec, vcc\n\t" \
    "s_cbranch_vccnz .Lrf4_%=\n\t" \
    "s_branch .Lok4_%=\n\t" \
    ".Lre5_%=:\n\t" \
    "s_mov_b32 s63, 0x20000\n\t" \
    "s_add_u32 s50, s46, 0xa000\n\t" \
    "s_add_u32 s51, s50, 0x1000\n\t" \
    ".Lrf5_%=:\n\t" \
    "buffer_load_dwordx4 v[144:147], v1, s[36:39], s50 offen sc1\n\t" \
    "buffer_load_dwordx4 v[148:151], v1, s[36:39], s50 offen offset:2048 sc1\n\t" \
    "buffer_load_dwordx4 v[152:155], v1, s[36:39], s51 offen sc1\n\t" \
    "buffer_load_dwordx4 v[156:159], v1, s[36:39], s51 offen offset:2048 sc1\n\t" \
    "s_waitcnt vmcnt(0)\n\t" \
    "v_max3_u32 v17, v144, v145, v146\n\t" \
    "v_max_u32 v17, v17, v147\n\t" \
    "v_max3_u32 v17, v148, v149, v17\n\t" \
    "v_max3_u32 v17, v150, v151, v17\n\t" \
    "v_max3_u32 v17, v152, v153, v17\n\t" \
    "v_max3_u32 v17, v154, v155, v17\n\t" \
    "v_max3_u32 v17, v156, v157, v17\n\t" \
    "v_max3_u32 v17, v158, v159, v17\n\t" \
    "v_cmp_eq_u32 vcc, -1, v17\n\t" \
    "s_sub_u32 s63, s63, 1\n\t" \
    "s_cbranch_scc1 .Lok5_%=\n\t" \
    "s_and_b64 vcc, exec, vcc\n\t" \
    "s_cbranch_vccnz .Lrf5_%=\n\t" \
    "s_branch .Lok5_%=\n\t" \
    ".Lre6_%=:\n\t" \
    "s_mov_b32 s63, 0x20000\n\t" \
    "s_add_u32 s50, s46, 0xc000\n\t" \
    "s_add_u32 s51, s50, 0x1000\n\t" \
    ".Lrf6_%=:\n\t" \
    "buffer_load_dwordx4 v[160:163], v1, s[36:39], s50 offen sc1\n\t" \
    "buffer_load_dwordx4 v[164:167], v1, s[36:39], s50 offen offset:2048 sc1\n\t" \
    "buffer_load_dwordx4 v[168:171], v1, s[36:39], s51 offen sc1\n\t" \
    "buffer_load_dwordx4 v[172:175], v1, s[36:39], s51 offen offset:2048 sc1\n\t" \
    "s_waitcnt vmcnt(0)\n\t" \
    "v_max3_u32 v17, v160, v161, v162\n\t" \
    "v_max_u32 v17, v17, v163\n\t" \
    "v_max3_u32 v17, v164, v165, v17\n\t" \
    "v_max3_u32 v17, v166, v167, v17\n\t" \
    "v_max3_u32 v17, v168, v169, v17\n\t" \
    "v_max3_u32 v17, v170, v171, v17\n\t" \
    "v_max3_u32 v17, v172, v173, v17\n\t" \
    "v_max3_u32 v17, v174, v175, v17\n\t" \
    "v_cmp_eq_u32 vcc, -1, v17\n\t" \
    "s_sub_u32 s63, s63, 1\n\t" \
    "s_cbranch_scc1 .Lok6_%=\n\t" \
    "s_and_b64 vcc, exec, vcc\n\t" \
    "s_cbranch_vccnz .Lrf6_%=\n\t" \
    "s_branch .Lok6_%=\n\t" \
    ".Lre7_%=:\n\t" \
    "s_mov_b32 s63, 0x20000\n\t" \
    "s_add_u32 s50, s46, 0xe000\n\t" \
    "s_add_u32 s51, s50, 0x1000\n\t" \
    ".Lrf7_%=:\n\t" \
    "buffer_load_dwordx4 v[176:179], v1, s[36:39], s50 offen sc1\n\t" \
    "buffer_load_dwordx4 v[180:183], v1, s[36:39], s50 offen offset:2048 sc1\n\t" \
    "buffer_load_dwordx4 v[184:187], v1, s[36:39], s51 offen sc1\n\t" \
    "buffer_load_dwordx4 v[188:191], v1, s[36:39], s51 offen offset:2048 sc1\n\t" \
    "s_waitcnt vmcnt(0)\n\t" \
    "v_max3_u32 v17, v176, v177, v178\n\t" \
    "v_max_u32 v17, v17, v179\n\t" \
    "v_max3_u32 v17, v180, v181, v17\n\t" \
    "v_max3_u32 v17, v182, v183, v17\n\t" \
    "v_max3_u32 v17, v184, v185, v17\n\t" \
    "v_max3_u32 v17, v186, v187, v17\n\t" \
    "v_max3_u32 v17, v188, v189, v17\n\t" \
    "v_max3_u32 v17, v190, v191, v17\n\t" \
    "v_cmp_eq_u32 vcc, -1, v17\n\t" \
    "s_sub_u32 s63, s63, 1\n\t" \
    "s_cbranch_scc1 .Lok7_%=\n\t" \
    "s_and_b64 vcc, exec, vcc\n\t" \
    "s_cbranch_vccnz .Lrf7_%=\n\t" \
    "s_branch .Lok7_%=\n\t" \
    ".Lre8_%=:\n\t" \
    "s_mov_b32 s63, 0x20000\n\t" \
    "s_add_u32 s50, s46, 0x10000\n\t" \
    "s_add_u32 s51, s50, 0x1000\n\t" \
    ".Lrf8_%=:\n\t" \
    "buffer_load_dwordx4 v[128:131], v1, s[36:39], s50 offen sc1\n\t" \
    "buffer_load_dwordx4 v[132:135], v1, s[36:39], s50 offen offset:2048 sc1\n\t" \
    "buffer_load_dwordx4 v[136:139], v1, s[36:39], s51 offen sc1\n\t" \
    "buffer_load_dwordx4 v[140:143], v1, s[36:39], s51 offen offset:2048 sc1\n\t" \
    "s_waitcnt vmcnt(0)\n\t" \
    "v_max3_u32 v17, v128, v129, v130\n\t" \
    "v_max_u32 v17, v17, v131\n\t" \
    "v_max3_u32 v17, v132, v133, v17\n\t" \
    "v_max3_u32 v17, v134, v135, v17\n\t" \
    "v_max3_u32 v17, v136, v137, v17\n\t" \
    "v_max3_u32 v17, v138, v139, v17\n\t" \
    "v_max3_u32 v17, v140, v141, v17\n\t" \
    "v_max3_u32 v17, v142, v143, v17\n\t" \
    "v_cmp_eq_u32 vcc, -1, v17\n\t" \
    "s_sub_u32 s63, s63, 1\n\t" \
    "s_cbranch_scc1 .Lok8_%=\n\t" \
    "s_and_b64 vcc, exec, vcc\n\t" \
    "s_cbranch_vccnz .Lrf8_%=\n\t" \
    "s_branch .Lok8_%=\n\t" \
    ".Lre9_%=:\n\t" \
    "s_mov_b32 s63, 0x20000\n\t" \
    "s_add_u32 s50, s46, 0x12000\n\t" \
    "s_add_u32 s51, s50, 0x1000\n\t" \
    ".Lrf9_%=:\n\t" \
    "buffer_load_dwordx4 v[144:147], v1, s[36:39], s50 offen sc1\n\t" \
    "buffer_load_dwordx4 v[148:151], v1, s[36:39], s50 offen offset:2048 sc1\n\t" \
    "buffer_load_dwordx4 v[152:155], v1, s[36:39], s51 offen sc1\n\t" \
    "buffer_load_dwordx4 v[156:159], v1, s[36:39], s51 offen offset:2048 sc1\n\t" \
    "s_waitcnt vmcnt(0)\n\t" \
    "v_max3_u32 v17, v144, v145, v146\n\t" \
    "v_max_u32 v17, v17, v147\n\t" \
    "v_max3_u32 v17, v148, v149, v17\n\t" \
    "v_max3_u32 v17, v150, v151, v17\n\t" \
    "v_max3_u32 v17, v152, v153, v17\n\t" \
    "v_max3_u32 v17, v154, v155, v17\n\t" \
    "v_max3_u32 v17, v156, v157, v17\n\t" \
    "v_max3_u32 v17, v158, v159, v17\n\t" \
    "v_cmp_eq_u32 vcc, -1, v17\n\t" \
    "s_sub_u32 s63, s63, 1\n\t" \
    "s_cbranch_scc1 .Lok9_%=\n\t" \
    "s_and_b64 vcc, exec, vcc\n\t" \
    "s_cbranch_vccnz .Lrf9_%=\n\t" \
    "s_branch .Lok9_%=\n\t" \
    ".Lre10_%=:\n\t" \
    "s_mov_b32 s63, 0x20000\n\t" \
    "s_add_u32 s50, s46, 0x14000\n\t" \
    "s_add_u32 s51, s50, 0x1000\n\t" \
    ".Lrf10_%=:\n\t" \
    "buffer_load_dwordx4 v[160:163], v1, s[36:39], s50 offen sc1\n\t" \
    "buffer_load_dwordx4 v[164:167], v1, s[36:39], s50 offen offset:2048 sc1\n\t" \
    "buffer_load_dwordx4 v[168:171], v1, s[36:39], s51 offen sc1\n\t" \
    "buffer_load_dwordx4 v[172:175], v1, s[36:39], s51 offen offset:2048 sc1\n\t" \
    "s_waitcnt vmcnt(0)\n\t" \
    "v_max3_u32 v17, v160, v161, v162\n\t" \
    "v_max_u32 v17, v17, v163\n\t" \
    "v_max3_u32 v17, v164, v165, v17\n\t" \
    "v_max3_u32 v17, v166, v167, v17\n\t" \
    "v_max3_u32 v17, v168, v169, v17\n\t" \
    "v_max3_u32 v17, v170, v171, v17\n\t" \
    "v_max3_u32 v17, v172, v173, v17\n\t" \
    "v_max3_u32 v17, v174, v175, v17\n\t" \
    "v_cmp_eq_u32 vcc, -1, v17\n\t" \
    "s_sub_u32 s63, s63, 1\n\t" \
    "s_cbranch_scc1 .Lok10_%=\n\t" \
    "s_and_b64 vcc, exec, vcc\n\t" \
    "s_cbranch_vccnz .Lrf10_%=\n\t" \
    "s_branch .Lok10_%=\n\t" \
    ".Lre11_%=:\n\t" \
    "s_mov_b32 s63, 0x20000\n\t" \
    "s_add_u32 s50, s46, 0x16000\n\t" \
    "s_add_u32 s51, s50, 0x1000\n\t" \
    ".Lrf11_%=:\n\t" \
    "buffer_load_dwordx4 v[176:179], v1, s[36:39], s50 offen sc1\n\t" \
    "buffer_load_dwordx4 v[180:183], v1, s[36:39], s50 offen offset:2048 sc1\n\t" \
    "buffer_load_dwordx4 v[184:187], v1, s[36:39], s51 offen sc1\n\t" \
    "buffer_load_dwordx4 v[188:191], v1, s[36:39], s51 offen offset:2048 sc1\n\t" \
    "s_waitcnt vmcnt(0)\n\t" \
    "v_max3_u32 v17, v176, v177, v178\n\t" \
    "v_max_u32 v17, v17, v179\n\t" \
    "v_max3_u32 v17, v180, v181, v17\n\t" \
    "v_max3_u32 v17, v182, v183, v17\n\t" \
    "v_max3_u32 v17, v184, v185, v17\n\t" \
    "v_max3_u32 v17, v186, v187, v17\n\t" \
    "v_max3_u32 v17, v188, v189, v17\n\t" \
    "v_max3_u32 v17, v190, v191, v17\n\t" \
    "v_cmp_eq_u32 vcc, -1, v17\n\t" \
    "s_sub_u32 s63, s63, 1\n\t" \
    "s_cbranch_scc1 .Lok11_%=\n\t" \
    "s_and_b64 vcc, exec, vcc\n\t" \
    "s_cbranch_vccnz .Lrf11_%=\n\t" \
    "s_branch .Lok11_%=\n\t" \
    ".Lre12_%=:\n\t" \
    "s_mov_b32 s63, 0x20000\n\t" \
    "s_add_u32 s50, s46, 0x18000\n\t" \
    "s_add_u32 s51, s50, 0x1000\n\t" \
    ".Lrf12_%=:\n\t" \
    "buffer_load_dwordx4 v[128:131], v1, s[36:39], s50 offen sc1\n\t" \
    "buffer_load_dwordx4 v[132:135], v1, s[36:39], s50 offen offset:2048 sc1\n\t" \
    "buffer_load_dwordx4 v[136:139], v1, s[36:39], s51 offen sc1\n\t" \
    "buffer_load_dwordx4 v[140:143], v1, s[36:39], s51 offen offset:2048 sc1\n\t" \
    "s_waitcnt vmcnt(0)\n\t" \
    "v_max3_u32 v17, v128, v129, v130\n\t" \
    "v_max_u32 v17, v17, v131\n\t" \
    "v_max3_u32 v17, v132, v133, v17\n\t" \
    "v_max3_u32 v17, v134, v135, v17\n\t" \
    "v_max3_u32 v17, v136, v137, v17\n\t" \
    "v_max3_u32 v17, v138, v139, v17\n\t" \
    "v_max3_u32 v17, v140, v141, v17\n\t" \
    "v_max3_u32 v17, v142, v143, v17\n\t" \
    "v_cmp_eq_u32 vcc, -1, v17\n\t" \
    "s_sub_u32 s63, s63, 1\n\t" \
    "s_cbranch_scc1 .Lok12_%=\n\t" \
    "s_and_b64 vcc, exec, vcc\n\t" \
    "s_cbranch_vccnz .Lrf12_%=\n\t" \
    "s_branch .Lok12_%=\n\t" \
    ".Lre13_%=:\n\t" \
    "s_mov_b32 s63, 0x20000\n\t" \
    "s_add_u32 s50, s46, 0x1a000\n\t" \
    "s_add_u32 s51, s50, 0x1000\n\t" \
    ".Lrf13_%=:\n\t" \
    "buffer_load_dwordx4 v[144:147], v1, s[36:39], s50 offen sc1\n\t" \
    "buffer_load_dwordx4 v[148:151], v1, s[36:39], s50 offen offset:2048 sc1\n\t" \
    "buffer_load_dwordx4 v[152:155], v1, s[36:39], s51 offen sc1\n\t" \
    "buffer_load_dwordx4 v[156:159], v1, s[36:39], s51 offen offset:2048 sc1\n\t" \
    "s_waitcnt vmcnt(0)\n\t" \
    "v_max3_u32 v17, v144, v145, v146\n\t" \
    "v_max_u32 v17, v17, v147\n\t" \
    "v_max3_u32 v17, v148, v149, v17\n\t" \
    "v_max3_u32 v17, v150, v151, v17\n\t" \
    "v_max3_u32 v17, v152, v153, v17\n\t" \
    "v_max3_u32 v17, v154, v155, v17\n\t" \
    "v_max3_u32 v17, v156, v157, v17\n\t" \
    "v_max3_u32 v17, v158, v159, v17\n\t" \
    "v_cmp_eq_u32 vcc, -1, v17\n\t" \
    "s_sub_u32 s63, s63, 1\n\t" \
    "s_cbranch_scc1 .Lok13_%=\n\t" \
    "s_and_b64 vcc, exec, vcc\n\t" \
    "s_cbranch_vccnz .Lrf13_%=\n\t" \
    "s_branch .Lok13_%=\n\t" \
    ".Lres_%=:\n\t" \
    "s_mov_b32 s63, 0x20000\n\t" \
    ".Lrfs_%=:\n\t" \
    "buffer_load_dwordx4 v[48:51], v18, s[36:39], s44 offen sc1\n\t" \
    "buffer_load_dwordx4 v[52:55], v18, s[36:39], s44 offen offset:256 sc1\n\t" \
    "s_waitcnt vmcnt(0)\n\t" \
    "v_max3_u32 v17, v48, v49, v50\n\t" \
    "v_max3_u32 v17, v51, v52, v17\n\t" \
    "v_max3_u32 v17, v53, v54, v17\n\t" \
    "v_max_u32 v17, v55, v17\n\t" \
    "v_cmp_eq_u32 vcc, -1, v17\n\t" \
    "s_sub_u32 s63, s63, 1\n\t" \
    "s_cbranch_scc1 .Loks_%=\n\t" \
    "s_and_b64 vcc, exec, vcc\n\t" \
    "s_cbranch_vccnz .Lrfs_%=\n\t" \
    "s_branch .Loks_%=\n\t" \
    ".Lffn_end:\n\t"

#define FFN_VCLOB "v1", "v2", "v3", "v4", "v5", "v6", "v7", "v8", "v9", "v10", "v11", "v12", "v13", "v14", "v15", "v16", "v17", "v18", "v19", "v20", "v21", "v22", "v23", "v24", "v25", "v26", "v27", "v28", "v29", "v30", "v31", "v32", "v33", "v34", "v35", "v36", "v37", "v38", "v39", "v40", "v41", "v42", "v43", "v44", "v45", "v46", "v47", "v48", "v49", "v50", "v51", "v52", "v53", "v54", "v55", "v56", "v57", "v58", "v59", "v60", "v61", "v62", "v63", "v64", "v65", "v66", "v67", "v68", "v69", "v70", "v71", "v72", "v73", "v74", "v75", "v76", "v77", "v78", "v79", "v80", "v81", "v82", "v83", "v84", "v85", "v86", "v87", "v88", "v89", "v90", "v91", "v92", "v93", "v94", "v95", "v96", "v97", "v98", "v99", "v100", "v101", "v102", "v103", "v104", "v105", "v106", "v107", "v108", "v109", "v110", "v111", "v112", "v113", "v114", "v115", "v116", "v117", "v118", "v119", "v120", "v121", "v122", "v123", "v124", "v125", "v126", "v127", "v128", "v129", "v130", "v131", "v132", "v133", "v134", "v135", "v136", "v137", "v138", "v139", "v140", "v141", "v142", "v143", "v144", "v145", "v146", "v147", "v148", "v149", "v150", "v151", "v152", "v153", "v154", "v155", "v156", "v157", "v158", "v159", "v160", "v161", "v162", "v163", "v164", "v165", "v166", "v167", "v168", "v169", "v170", "v171", "v172", "v173", "v174", "v175", "v176", "v177", "v178", "v179", "v180", "v181", "v182", "v183", "v184", "v185", "v186", "v187", "v188", "v189", "v190", "v191", "v192", "v193", "v194", "v195", "v196", "v197", "v198", "v199", "v200", "v201", "v202", "v203", "v204", "v205", "v206", "v207", "v208", "v209", "v210", "v211", "v212", "v213", "v214", "v215", "v216", "v217", "v218", "v219", "v220", "v221", "v222", "v223", "v224", "v225", "v226", "v227", "v228", "v229", "v230", "v231", "v232", "v233", "v234", "v235", "v236", "v237", "v238", "v239", "v240", "v241", "v242", "v243", "v244", "v245", "v246", "v247", "v248", "v249", "v250", "v251", "v252", "v253", "v254", "v255"
#define FFN_SCLOB "s33", "s34", "s35", "s36", "s37", "s38", "s39", "s40", "s41", "s42", "s43", "s44", "s45", "s46", "s47", "s48", "s49", "s50", "s51", "s52", "s53", "s54", "s55", "s56", "s57", "s58", "s59", "s60", "s61", "s62", "s63", "s92"

__device__ const __attribute__((aligned(16))) u16 ztab[1032] = {ZTAB};

__global__ __launch_bounds__(512, 2) void ssm_kernel(const u16* x, const u16* Are, const u16* Aim, const u16* Bre, const u16* Bim,
    const u16* Cre, const u16* Cim, const u16* ldt, const u16* dsk, const u16* wg, const u16* wu, const u16* wd, char* ws) {
  __shared__ __attribute__((aligned(16))) char lds[A_LDS];
  u32 tid = threadIdx.x;
  const u32 w = __builtin_amdgcn_readfirstlane(tid >> 6);
  asm volatile(SSM_ASM
               : "+v"(tid)
               : "s"(w), "s"(x), "s"(Are), "s"(Aim), "s"(Bre), "s"(Bim), "s"(Cre), "s"(Cim), "s"(ldt), "s"(dsk), "s"(wg), "s"(wu), "s"(wd),
                 "s"(ws), "s"(blockIdx.x), "s"(LDS_ADDR(lds)), "s"(ztab)
               : "memory", "vcc", "scc", SSM_SCLOB, SSM_VCLOB);
}

__global__ __launch_bounds__(512, 2) void ffn_kernel(const u16* wd, char* ws, u16* out) {
  __shared__ __attribute__((aligned(16))) char lds[9 * 16384 + 1024 + 8192];
  u32 tid = threadIdx.x;
  const u32 w = __builtin_amdgcn_readfirstlane(tid >> 6);
  asm volatile(FFN_ASM
               : "+v"(tid)
               : "s"(w), "s"(wd), "s"(ws), "s"(out), "s"(blockIdx.x), "s"(LDS_ADDR(lds))
               : "memory", "vcc", "scc", FFN_SCLOB, FFN_VCLOB);
}

extern "C" void kernel_launch(void* const* d_in, const int* in_sizes, int n_in, void* d_out, int out_size, void* d_ws, size_t ws_size,
                              hipStream_t stream) {
  char* ws = (char*)d_ws;
  ssm_kernel<<<256, 512, 0, stream>>>((const u16*)d_in[0], (const u16*)d_in[1], (const u16*)d_in[2], (const u16*)d_in[3], (const u16*)d_in[4],
                                      (const u16*)d_in[5], (const u16*)d_in[6], (const u16*)d_in[7], (const u16*)d_in[8], (const u16*)d_in[10],
                                      (const u16*)d_in[11], (const u16*)d_in[12], ws);
  ffn_kernel<<<256, 512, 0, stream>>>((const u16*)d_in[12], ws, (u16*)d_out);
}
```
